```python
import torch

def _softmax(x, axis=-1):
    return torch.softmax(x, dim=axis)
def _gelu(x):
    return torch.nn.functional.gelu(x, approximate="tanh")
def _silu(x):
    return torch.nn.functional.silu(x)
def _sigmoid(x):
    return torch.sigmoid(x)
def _layernorm(x, axis=-1, eps=1e-5):
    m = x.mean(dim=axis, keepdim=True)
    v = x.var(dim=axis, keepdim=True, unbiased=False)
    return (x - m) / torch.sqrt(v + eps)
def _scatter_add(tgt, idx, src, axis=0):
    return tgt.clone().index_add_(axis, idx, src)
def _segment_sum(src, seg, n):
    out = torch.zeros((n,) + src.shape[1:], dtype=src.dtype)
    return out.index_add_(0, seg, src)
def _pad(x, pw):
    flat = tuple(v for pair in reversed(pw) for v in pair)
    return torch.nn.functional.pad(x, flat)

kernel_name = 'sequential_preLN__ssm_conv_h3__glu_gated_reglu__prompt'

def setup_inputs():

    _w = (lambda a: (lambda t: t.to(torch.bfloat16) if t.is_floating_point() else t)(torch.as_tensor(a)))
    def _A_setup():
        B, S, D = 1, 2048, 512
        ks = 4
        return {
            "q": _w((lambda *s: torch.randn(*s, dtype=torch.bfloat16) * 0.05)(B, S, D)),
            "k": _w((lambda *s: torch.randn(*s, dtype=torch.bfloat16) * 0.2)(B, S, D)),
            "v": _w((lambda *s: torch.randn(*s, dtype=torch.bfloat16) * 0.2)(B, S, D)),
            "shift_k": _w((lambda *s: torch.randn(*s, dtype=torch.bfloat16) * 0.2)(D, ks) * 0.5),
            "ssm_k": _w((lambda *s: torch.randn(*s, dtype=torch.bfloat16) * 0.4)(D, S) * (1.0 / S ** 0.5)),
            "ssm_D": _w((lambda *s: torch.randn(*s, dtype=torch.bfloat16) * 0.2)(D)),
        }
    _A_in = _A_setup()

    def _B_setup():
        B, S, D, F = 1, 2048, 512, 1792
        return {
            "x": _w((lambda *s: torch.randn(*s, dtype=torch.bfloat16) * 0.05)(B, S, D)),
            "W_gate": _w((lambda *s: torch.randn(*s, dtype=torch.bfloat16) * 0.2)(D, F) * (2.0 / D) ** 0.5),
            "W_up": _w((lambda *s: torch.randn(*s, dtype=torch.bfloat16) * 0.2)(D, F) * (2.0 / D) ** 0.5),
            "W_down": _w((lambda *s: torch.randn(*s, dtype=torch.bfloat16) * 0.2)(F, D) * (1.0 / F) ** 0.5),
        }
    _B_in = _B_setup()

    return {
        "A_q": _A_in["q"],
        "A_k": _A_in["k"],
        "A_v": _A_in["v"],
        "A_shift_k": _A_in["shift_k"],
        "A_ssm_k": _A_in["ssm_k"],
        "A_ssm_D": _A_in["ssm_D"],
        "B_x": _B_in["x"],
        "B_W_gate": _B_in["W_gate"],
        "B_W_up": _B_in["W_up"],
        "B_W_down": _B_in["W_down"],
    }

def reference(A_q, A_k, A_v, A_shift_k, A_ssm_k, A_ssm_D, B_x, B_W_gate, B_W_up, B_W_down):
    def _A_ref(q, k, v, shift_k, ssm_k, ssm_D):
        B, S, D = q.shape
        ks = shift_k.shape[-1]
        k_pad = _pad(k, ((0, 0), (ks - 1, 0), (0, 0)))
        k_shift = torch.zeros_like(k)
        for t in range(ks):
            k_shift = k_shift + k_pad[:, t:t + S, :] * shift_k[:, ks - 1 - t][None, None, :]
        kv = k_shift * v
        L = 2 * S
        kv_pad = _pad(kv, ((0, 0), (0, S), (0, 0)))
        kf_pad = _pad(ssm_k, ((0, 0), (0, S)))
        kv_f = (lambda x, axes=(-2,-1): torch.fft.rfft2(x.float(), dim=axes))(kv_pad[:, None, :, :], axes=(1, 2))
        kf_f = (lambda x, axes=(-2,-1): torch.fft.rfft2(x.float(), dim=axes))(kf_pad[:, None, :], axes=(1, 2))
        kf_f = (lambda x, *d: x.permute(*d))(kf_f, 1, 2, 0)
        y_f = kv_f * kf_f[None, :, :, :]
        y = (lambda x, s, axes=(-2,-1): torch.fft.irfft2(x, s=s, dim=axes).to(torch.bfloat16))(y_f, (1, L), axes=(1, 2))[:, 0, :S, :]
        y = y + kv * ssm_D[None, None, :]
        return q * y

    def _B_ref(x, W_gate, W_up, W_down):
        gate = torch.einsum("bsd,df->bsf", x, W_gate)
        up = torch.einsum("bsd,df->bsf", x, W_up)
        gate = gate * (lambda x, dt: x.to(dt))(gate > 0, gate.dtype)
        h = gate * up
        return torch.einsum("bsf,fd->bsd", h, W_down)

    x = A_q
    h = x + _A_ref(_layernorm(x, axis=-1), A_k, A_v, A_shift_k, A_ssm_k, A_ssm_D)
    h2 = h + _B_ref(_layernorm(h, axis=-1), B_W_gate, B_W_up, B_W_down)
    return h2
```

```cpp
#include <hip/hip_runtime.h>
#include <hip/hip_bf16.h>
#include <stdint.h>

typedef unsigned short u16;
typedef __attribute__((ext_vector_type(8))) short bf16x8;
typedef __attribute__((ext_vector_type(4))) short s16x4;
typedef __attribute__((ext_vector_type(16))) float f32x16;
typedef __attribute__((ext_vector_type(4))) float f32x4;
typedef __attribute__((ext_vector_type(4))) unsigned u32x4;
typedef __attribute__((ext_vector_type(2))) unsigned u32x2;
typedef __attribute__((ext_vector_type(2))) __bf16 bf16x2_t;
typedef __attribute__((ext_vector_type(4))) short v4i16_t;

#define DEV __device__ __forceinline__

static constexpr int S_ = 2048, D_ = 512, F_ = 1792;

DEV float bf2f(unsigned x) { return __uint_as_float(x << 16); }
DEV float bflo(unsigned x) { return __uint_as_float(x << 16); }
DEV float bfhi(unsigned x) { return __uint_as_float(x & 0xffff0000u); }
DEV unsigned f2bf(float x) { return (unsigned)__builtin_bit_cast(u16, (__bf16)x); }
DEV float rbf(float x) { return (float)(__bf16)x; }
DEV unsigned pack2(float lo, float hi) { return f2bf(lo) | (f2bf(hi) << 16); }

__global__ __launch_bounds__(256) void k1_prep(const u16* __restrict__ q, const u16* __restrict__ k,
                                               const u16* __restrict__ v, const u16* __restrict__ shk,
                                               const u16* __restrict__ wg, const u16* __restrict__ wu,
                                               u16* __restrict__ kvR, u16* __restrict__ xT,
                                               float* __restrict__ qm, float* __restrict__ qden,
                                               float* __restrict__ cs) {
  __shared__ __attribute__((aligned(16))) unsigned char smem[2 * 64 * 66 * 2];
  const int b = blockIdx.x, t = threadIdx.x;
  if (b < 256) {
    u16* kvs = (u16*)smem;
    u16* xs = (u16*)(smem + 64 * 66 * 2);
    const int s0 = (b & 31) * 64, d0 = (b >> 5) * 64;
    const int c = t & 7, r0 = t >> 3;
    u32x4 wv[4];
#pragma unroll
    for (int i = 0; i < 4; ++i) wv[i] = *(const u32x4*)(shk + (d0 + 8 * c) * 4 + 8 * i);
#pragma unroll
    for (int p = 0; p < 2; ++p) {
      const int r = r0 + 32 * p, s = s0 + r;
      u32x4 kr[4];
#pragma unroll
      for (int j = 0; j < 4; ++j) {
        const int ss = s - 3 + j;
        u32x4 z = {0u, 0u, 0u, 0u};
        kr[j] = (ss >= 0) ? *(const u32x4*)(k + ss * D_ + d0 + 8 * c) : z;
      }
      const u32x4 vv = *(const u32x4*)(v + s * D_ + d0 + 8 * c);
      const u32x4 qq = *(const u32x4*)(q + s * D_ + d0 + 8 * c);
#pragma unroll
      for (int e = 0; e < 8; ++e) {
        const unsigned wa = wv[e >> 1][(e & 1) * 2], wb = wv[e >> 1][(e & 1) * 2 + 1];
        const float w0 = bflo(wa), w1 = bfhi(wa), w2 = bflo(wb), w3 = bfhi(wb);
        float kf[4];
#pragma unroll
        for (int j = 0; j < 4; ++j) kf[j] = (e & 1) ? bfhi(kr[j][e >> 1]) : bflo(kr[j][e >> 1]);
        float ks = rbf(kf[0] * w3);
        ks = rbf(ks + rbf(kf[1] * w2));
        ks = rbf(ks + rbf(kf[2] * w1));
        ks = rbf(ks + rbf(kf[3] * w0));
        const float vf = (e & 1) ? bfhi(vv[e >> 1]) : bflo(vv[e >> 1]);
        const unsigned kvb = f2bf(ks * vf);
        const unsigned qb = (e & 1) ? (qq[e >> 1] >> 16) : (qq[e >> 1] & 0xffffu);
        kvs[(8 * c + e) * 66 + (63 - r)] = (u16)kvb;
        xs[(8 * c + e) * 66 + r] = (u16)qb;
      }
    }
    __syncthreads();
#pragma unroll
    for (int p = 0; p < 2; ++p) {
      const int ch = r0 + 32 * p;
      const unsigned* a = (const unsigned*)(kvs + ch * 66 + 8 * c);
      const unsigned* bq = (const unsigned*)(xs + ch * 66 + 8 * c);
      u32x4 o1 = {a[0], a[1], a[2], a[3]};
      u32x4 o2 = {bq[0], bq[1], bq[2], bq[3]};
      *(u32x4*)(kvR + (d0 + ch) * S_ + (1984 - s0) + 8 * c) = o1;
      *(u32x4*)(xT + (d0 + ch) * S_ + s0 + 8 * c) = o2;
    }
  } else if (b < 512) {
    const int lane = t & 63, w = t >> 6;
#pragma unroll
    for (int i = 0; i < 2; ++i) {
      const int row = (b - 256) * 8 + w * 2 + i;
      const u32x4 x = *(const u32x4*)(q + row * D_ + lane * 8);
      float f[8];
#pragma unroll
      for (int j = 0; j < 4; ++j) { f[2 * j] = bflo(x[j]); f[2 * j + 1] = bfhi(x[j]); }
      float sum = 0.f;
#pragma unroll
      for (int j = 0; j < 8; ++j) sum += f[j];
#pragma unroll
      for (int o = 32; o >= 1; o >>= 1) sum += __shfl_xor(sum, o);
      const float mean = sum * (1.0f / D_);
      float sq = 0.f;
#pragma unroll
      for (int j = 0; j < 8; ++j) { const float d = f[j] - mean; sq += d * d; }
#pragma unroll
      for (int o = 32; o >= 1; o >>= 1) sq += __shfl_xor(sq, o);
      const float var = rbf(sq * (1.0f / D_));
      if (lane == 0) {
        qm[row] = rbf(mean);
        qden[row] = rbf(sqrtf(rbf(var + 1e-5f)));
      }
    }
  } else {
    float* red = (float*)smem;
    const int bb = b - 512, mat = bb / 28, tile = bb % 28;
    const u16* W = mat ? wu : wg;
    const int c = t & 7, g = t >> 3;
    float acc[8];
#pragma unroll
    for (int j = 0; j < 8; ++j) acc[j] = 0.f;
#pragma unroll
    for (int i = 0; i < 16; ++i) {
      const u32x4 x = *(const u32x4*)(W + (g * 16 + i) * F_ + tile * 64 + 8 * c);
#pragma unroll
      for (int j = 0; j < 4; ++j) { acc[2 * j] += bflo(x[j]); acc[2 * j + 1] += bfhi(x[j]); }
    }
#pragma unroll
    for (int j = 0; j < 8; ++j) red[g * 64 + 8 * c + j] = acc[j];
    __syncthreads();
    if (t < 64) {
      float s = 0.f;
#pragma unroll
      for (int i = 0; i < 32; ++i) s += red[i * 64 + t];
      cs[mat * F_ + tile * 64 + t] = s;
    }
  }
}

static constexpr int K2_COPY = 4160;
static constexpr int K2_KC = 8 * K2_COPY;
static constexpr int K2_XR = 96 * 80;
static constexpr int K2_CH = K2_KC + K2_XR;

DEV void k2_write_copies(unsigned char* kc, int unit, u32x4 cur, u32x4 nxt) {
  unsigned w[8] = {cur[0], cur[1], cur[2], cur[3], nxt[0], nxt[1], nxt[2], nxt[3]};
#pragma unroll
  for (int sh = 1; sh <= 8; ++sh) {
    u32x4 o;
    const int a = sh >> 1;
    if (sh & 1) {
#pragma unroll
      for (int i = 0; i < 4; ++i) o[i] = __builtin_amdgcn_alignbit(w[a + i + 1], w[a + i], 16);
    } else {
#pragma unroll
      for (int i = 0; i < 4; ++i) o[i] = w[a + i];
    }
    *(u32x4*)(kc + (sh - 1) * K2_COPY + unit * 16) = o;
  }
}

__global__ __launch_bounds__(256) void k2_conv(const u16* __restrict__ ssmk, const u16* __restrict__ ssmD,
                                               const u16* __restrict__ kvR, const u16* __restrict__ xT,
                                               const float* __restrict__ qm, const float* __restrict__ qden,
                                               u16* __restrict__ h) {
  __shared__ __attribute__((aligned(16))) unsigned char smem[2 * K2_CH];
  const int t = threadIdx.x, lane = t & 63, w = t >> 6;
  const int d0 = blockIdx.x * 2;
  const u32x4 z4 = {0u, 0u, 0u, 0u};
#pragma unroll
  for (int ch = 0; ch < 2; ++ch) {
    unsigned char* base = smem + ch * K2_CH;
    const u32x4 xv = *(const u32x4*)(kvR + (d0 + ch) * S_ + t * 8);
    *(u32x4*)(base + K2_KC + (t >> 2) * 80 + (t & 3) * 16) = xv;
    const u16* kk = ssmk + (d0 + ch) * S_;
    const int vv = t - 4;
    const u32x4 cur = (vv >= 0) ? *(const u32x4*)(kk + vv * 8) : z4;
    const u32x4 nxt = (vv + 1 >= 0) ? *(const u32x4*)(kk + (vv + 1) * 8) : z4;
    k2_write_copies(base, t, cur, nxt);
    if (t < 4) {
      const int v2 = 252 + t;
      const u32x4 c2 = *(const u32x4*)(kk + v2 * 8);
      const u32x4 n2 = (v2 + 1 <= 255) ? *(const u32x4*)(kk + (v2 + 1) * 8) : z4;
      k2_write_copies(base, v2 + 4, c2, n2);
    }
  }
  {
    const int ch = t >> 7, idx = t & 127;
    *(u32x4*)(smem + ch * K2_CH + K2_KC + (64 + (idx >> 2)) * 80 + (idx & 3) * 16) = z4;
  }
  __syncthreads();

  const int ch = w >> 1, half = w & 1;
  const int il = lane & 7, ih = (lane >> 3) & 3, hh = lane >> 5, col = lane & 31;
  const unsigned char* kcb = smem + ch * K2_CH + il * K2_COPY + (2 + ih + hh + 64 * half) * 16;
  const unsigned char* xb = smem + ch * K2_CH + K2_KC + (63 - col + 16 * half) * 80 + (16 + 8 * hh) * 2;
  f32x16 acc0, acc1;
#pragma unroll
  for (int i = 0; i < 16; ++i) { acc0[i] = 0.f; acc1[i] = 0.f; }
#pragma unroll 4
  for (int c = 0; c < 16; ++c) {
#pragma unroll
    for (int s = 0; s < 2; ++s) {
      const bf16x8 a0 = *(const bf16x8*)(kcb + (4 * c - 2 * s) * 16);
      const bf16x8 a1 = *(const bf16x8*)(kcb + (4 * c - 2 * s + 128) * 16);
      const bf16x8 b0 = *(const bf16x8*)(xb + c * 80 - 32 * s);
      const bf16x8 b1 = *(const bf16x8*)(xb + (c - 32) * 80 - 32 * s);
      acc0 = __builtin_amdgcn_mfma_f32_32x32x16_bf16(a0, b0, acc0, 0, 0, 0);
      acc1 = __builtin_amdgcn_mfma_f32_32x32x16_bf16(a0, b1, acc1, 0, 0, 0);
      acc1 = __builtin_amdgcn_mfma_f32_32x32x16_bf16(a1, b0, acc1, 0, 0, 0);
    }
  }
  __syncthreads();
  {
    float* red = (float*)(smem + ch * K2_CH);
    float* dst = red + (half ^ 1) * 1024;
#pragma unroll
    for (int i = 0; i < 16; ++i) dst[i * 64 + lane] = half ? acc0[i] : acc1[i];
  }
  __syncthreads();
  f32x16 acc = half ? acc1 : acc0;
  {
    const float* src = (const float*)(smem + ch * K2_CH) + half * 1024;
#pragma unroll
    for (int i = 0; i < 16; ++i) acc[i] += src[i * 64 + lane];
  }
  const int d = d0 + ch;
  const float Dd = bf2f(ssmD[d]);
  u16* hs = (u16*)(smem + 8192);
#pragma unroll
  for (int qd = 0; qd < 4; ++qd) {
    const int n0 = 1024 * half + 32 * col + 8 * qd + 4 * hh;
    const int t0 = 2044 - n0;
    const u32x2 kvp = *(const u32x2*)(smem + ch * K2_CH + K2_KC + (t0 >> 5) * 80 + (t0 & 31) * 2);
    const u32x2 xq = *(const u32x2*)(xT + d * S_ + n0);
    const f32x4 mm = *(const f32x4*)(qm + n0);
    const f32x4 dd = *(const f32x4*)(qden + n0);
    float kvf[4] = {bfhi(kvp[1]), bflo(kvp[1]), bfhi(kvp[0]), bflo(kvp[0])};
    float xf[4] = {bflo(xq[0]), bfhi(xq[0]), bflo(xq[1]), bfhi(xq[1])};
#pragma unroll
    for (int r = 0; r < 4; ++r) {
      const float y = rbf(acc[qd * 4 + r]);
      const float y2 = rbf(y + rbf(kvf[r] * Dd));
      const float qn = rbf(rbf(xf[r] - mm[r]) / dd[r]);
      const float hv = xf[r] + rbf(qn * y2);
      hs[(n0 + r) * 2 + ch] = (u16)f2bf(hv);
    }
  }
  __syncthreads();
#pragma unroll
  for (int i = 0; i < 8; ++i) {
    const int n = t + 256 * i;
    *(unsigned*)(h + n * D_ + d0) = *(const unsigned*)(hs + n * 2);
  }
}

static constexpr int K3_A = 0, K3_G = 16384, K3_U = 16384 + 14336 + 128, K3_STAGE = 45184;

DEV void glds16(const void* gsrc, unsigned lds_dst) {
  unsigned keep;
  asm volatile("s_mov_b32 %0, m0\n\ts_mov_b32 m0, %2\n\ts_nop 0\n\tglobal_load_lds_dwordx4 %1, off\n\ts_mov_b32 m0, %0"
               : "=&s"(keep) : "v"(gsrc), "s"(lds_dst) : "memory");
}
#define GLDS(src, dst) glds16((const void*)(src), (unsigned)(size_t)(dst))
#define WAITV(n) asm volatile("s_waitcnt vmcnt(" #n ")" ::: "memory")

typedef __attribute__((address_space(3))) unsigned char lds_u8;

DEV s16x4 tr_read(const lds_u8* p) {
  return __builtin_bit_cast(s16x4, __builtin_amdgcn_ds_read_tr16_b64_v4i16((__attribute__((address_space(3))) v4i16_t*)p));
}

__global__ __launch_bounds__(256) void k3_gemm1(const u16* __restrict__ h, const u16* __restrict__ wg,
                                                const u16* __restrict__ wu, const float* __restrict__ cs,
                                                u16* __restrict__ hh) {
  __shared__ __attribute__((aligned(16))) unsigned char smem_[3 * K3_STAGE + 1024];
  lds_u8* smem = (lds_u8*)smem_;
  const int t = threadIdx.x, lane = t & 63;
  const int w = __builtin_amdgcn_readfirstlane(t >> 6);
  const int b = blockIdx.x, x = b & 7, idx = b >> 3;
  const int mt = (x & 1) * 8 + (idx & 7), ft = (x >> 1) * 4 + (idx >> 3);
  const int m0 = mt * 128, f0 = ft * 112;
  float* csl = (float*)(smem_ + 3 * K3_STAGE);
  if (t < 224) csl[t] = cs[(t >= 112 ? F_ - 112 : 0) + f0 + t];

  const u16* asrc[4];
#pragma unroll
  for (int i = 0; i < 4; ++i) {
    const int p = (w * 4 + i) * 64 + lane, row = p >> 3, slot = p & 7;
    const int chunk = slot ^ ((row >> 1) & 7);
    asrc[i] = h + (m0 + row) * D_ + chunk * 8;
  }
  const u16* wsrc[7];
#pragma unroll
  for (int i = 0; i < 7; ++i) {
    const int j = w * 7 + i, mat = j >= 14, jj = j - 14 * mat;
    const int p = jj * 64 + lane, row = p / 14, part = p % 14;
    wsrc[i] = (mat ? wu : wg) + row * F_ + f0 + part * 8;
  }
  auto issue = [&](int kt, int buf) {
    lds_u8* sb = smem + buf * K3_STAGE;
#pragma unroll
    for (int i = 0; i < 4; ++i) GLDS(asrc[i] + kt * 64, sb + K3_A + (w * 4 + i) * 1024);
#pragma unroll
    for (int i = 0; i < 7; ++i) {
      const int j = w * 7 + i, mat = j >= 14, jj = j - 14 * mat;
      GLDS(wsrc[i] + kt * 64 * F_, sb + (mat ? K3_U : K3_G) + jj * 1024);
    }
  };
  __syncthreads();
  issue(0, 0);
  issue(1, 1);

  f32x16 acc[7];
#pragma unroll
  for (int j = 0; j < 7; ++j)
#pragma unroll
    for (int i = 0; i < 16; ++i) acc[j][i] = 0.f;
  float sum = 0.f, sq = 0.f;
  const int hf = lane >> 5, r32 = lane & 31;
  const int arow = w * 32 + r32;
  const int aoff = K3_A + arow * 128;
  const int asw = (arow >> 1) & 7;
  const int woff = ((lane >> 4) & 1 ? K3_U : K3_G) + (8 * hf + ((lane & 15) >> 2)) * 224 + (lane & 3) * 8;
  const bf16x2_t ones = {(__bf16)1.0f, (__bf16)1.0f};

  for (int kt = 0; kt < 8; ++kt) {
    if (kt < 7) WAITV(11); else WAITV(0);
    __builtin_amdgcn_s_barrier();
    if (kt + 2 < 8) issue(kt + 2, (kt + 2) % 3);
    const lds_u8* sb = smem + (kt % 3) * K3_STAGE;
#pragma unroll
    for (int ks = 0; ks < 4; ++ks) {
      const bf16x8 bfrag = *(const __attribute__((address_space(3))) bf16x8*)(sb + aoff + (((2 * ks + hf) ^ asw) * 16));
      const u32x4 bu = __builtin_bit_cast(u32x4, bfrag);
#pragma unroll
      for (int i = 0; i < 4; ++i) {
        const unsigned xi = bu[i];
        const bf16x2_t xx = __builtin_bit_cast(bf16x2_t, xi);
        sum = __builtin_amdgcn_fdot2_f32_bf16(xx, ones, sum, false);
        sq = __builtin_amdgcn_fdot2_f32_bf16(xx, xx, sq, false);
      }
#pragma unroll
      for (int j = 0; j < 7; ++j) {
        const s16x4 lo = tr_read(sb + woff + ks * 16 * 224 + j * 32);
        const s16x4 hi = tr_read(sb + woff + (ks * 16 + 4) * 224 + j * 32);
        const bf16x8 afrag = {lo[0], lo[1], lo[2], lo[3], hi[0], hi[1], hi[2], hi[3]};
        acc[j] = __builtin_amdgcn_mfma_f32_32x32x16_bf16(afrag, bfrag, acc[j], 0, 0, 0);
      }
    }
  }
  sum += __shfl_xor(sum, 32);
  sq += __shfl_xor(sq, 32);
  const float mean = sum * (1.0f / D_);
  const float var = rbf(fmaxf(sq * (1.0f / D_) - mean * mean, 0.f));
  const float mb = rbf(mean);
  const float inv = 1.0f / rbf(sqrtf(rbf(var + 1e-5f)));
  u16* orow = hh + (size_t)(m0 + arow) * F_ + f0 + 8 * hf;
#pragma unroll
  for (int j = 0; j < 7; ++j) {
    float o[8];
#pragma unroll
    for (int g = 0; g < 2; ++g) {
      const f32x4 cg = *(const f32x4*)(csl + 16 * j + 8 * g + 4 * hf);
      const f32x4 cu = *(const f32x4*)(csl + 112 + 16 * j + 8 * g + 4 * hf);
#pragma unroll
      for (int r = 0; r < 4; ++r) {
        const float gt = rbf((acc[j][4 * g + r] - mb * cg[r]) * inv);
        const float up = rbf((acc[j][8 + 4 * g + r] - mb * cu[r]) * inv);
        o[4 * g + r] = (gt > 0.f ? gt : 0.f) * up;
      }
    }
    unsigned a0 = pack2(o[0], o[1]), a1 = pack2(o[2], o[3]);
    unsigned b0 = pack2(o[4], o[5]), b1 = pack2(o[6], o[7]);
    auto s0 = __builtin_amdgcn_permlane32_swap(a0, b0, false, false);
    auto s1 = __builtin_amdgcn_permlane32_swap(a1, b1, false, false);
    u32x4 ov = {s0[0], s1[0], s0[1], s1[1]};
    *(u32x4*)(orow + 16 * j) = ov;
  }
}

static constexpr int K4_SLOT = 8192, K4_NSLOT = 4, K4_WAVE = K4_SLOT * K4_NSLOT;

__global__ __launch_bounds__(256) void k4_gemm2(const u16* __restrict__ hh, const u16* __restrict__ wd,
                                                const u16* __restrict__ h, u16* __restrict__ out) {
  __shared__ __attribute__((aligned(16))) unsigned char smem_[4 * K4_WAVE];
  lds_u8* smem = (lds_u8*)smem_;
  const int t = threadIdx.x, lane = t & 63;
  const int w = __builtin_amdgcn_readfirstlane(t >> 6);
  const int b = blockIdx.x, x = b & 7, idx = b >> 3;
  const int mt = x * 4 + (idx & 3), nt = idx >> 2;
  const int m0 = mt * 64, n0 = nt * 64;
  lds_u8* wb = smem + w * K4_WAVE;
  const int kbase = w * 448;
  const u16* asrc[4];
  const u16* bsrc[4];
#pragma unroll
  for (int i = 0; i < 4; ++i) {
    const int p = i * 64 + lane, row = p >> 2, slot = p & 3;
    const int chunk = slot ^ ((row >> 2) & 3);
    asrc[i] = hh + (size_t)(m0 + row) * F_ + kbase + chunk * 8;
    const int rb = p >> 3, sb = p & 7;
    const int cb = sb ^ (((rb >> 1) & 1) << 2);
    bsrc[i] = wd + (size_t)(kbase + rb) * D_ + n0 + cb * 8;
  }
  auto issue = [&](int st) {
    lds_u8* sb = wb + (st & 3) * K4_SLOT;
#pragma unroll
    for (int i = 0; i < 4; ++i) GLDS(asrc[i] + st * 32, sb + i * 1024);
#pragma unroll
    for (int i = 0; i < 4; ++i) GLDS(bsrc[i] + (size_t)st * 32 * D_, sb + 4096 + i * 1024);
  };
  issue(0); issue(1); issue(2);
  f32x16 acc[2][2];
#pragma unroll
  for (int i = 0; i < 2; ++i)
#pragma unroll
    for (int j = 0; j < 2; ++j)
#pragma unroll
      for (int e = 0; e < 16; ++e) acc[i][j][e] = 0.f;
  const int hf = lane >> 5, r32 = lane & 31;
  const int a_row0 = r32;
  const int asw = (r32 >> 2) & 3;
  const int bq = (lane & 15) >> 2;
  const int brow = 8 * hf + bq;
  const int bsw = ((bq >> 1) & 1) << 2;
  const int bcol = 16 * ((lane >> 4) & 1) + 4 * (lane & 3);
  const int boff0 = 4096 + brow * 128 + (((bcol >> 3) ^ bsw) * 16) + (bcol & 7) * 2;
  const int boff1 = 4096 + brow * 128 + ((((bcol + 32) >> 3) ^ bsw) * 16) + (bcol & 7) * 2;

  for (int st = 0; st < 14; ++st) {
    if (st < 12) WAITV(16); else if (st == 12) WAITV(8); else WAITV(0);
    const lds_u8* sb = wb + (st & 3) * K4_SLOT;
    bf16x8 af[2][2], bf[2][2];
#pragma unroll
    for (int ks = 0; ks < 2; ++ks) {
#pragma unroll
      for (int mb = 0; mb < 2; ++mb)
        af[ks][mb] = *(const __attribute__((address_space(3))) bf16x8*)(sb + (a_row0 + 32 * mb) * 64 + (((2 * ks + hf) ^ asw) * 16));
#pragma unroll
      for (int nb = 0; nb < 2; ++nb) {
        const int bo = nb ? boff1 : boff0;
        const s16x4 lo = tr_read(sb + bo + ks * 16 * 128);
        const s16x4 hi = tr_read(sb + bo + (ks * 16 + 4) * 128);
        bf[ks][nb] = (bf16x8){lo[0], lo[1], lo[2], lo[3], hi[0], hi[1], hi[2], hi[3]};
      }
    }
#pragma unroll
    for (int ks = 0; ks < 2; ++ks)
#pragma unroll
      for (int mb = 0; mb < 2; ++mb)
#pragma unroll
        for (int nb = 0; nb < 2; ++nb)
          acc[mb][nb] = __builtin_amdgcn_mfma_f32_32x32x16_bf16(af[ks][mb], bf[ks][nb], acc[mb][nb], 0, 0, 0);
    if (st + 3 < 14) issue(st + 3);
  }
  __syncthreads();
  float* red = (float*)smem_;
#pragma unroll
  for (int mb = 0; mb < 2; ++mb)
#pragma unroll
    for (int nb = 0; nb < 2; ++nb)
#pragma unroll
      for (int e = 0; e < 16; ++e) {
        const int row = 32 * mb + (e & 3) + 8 * (e >> 2) + 4 * hf;
        red[w * 64 * 68 + row * 68 + 32 * nb + r32] = acc[mb][nb][e];
      }
  __syncthreads();
  {
    const int row = t >> 2, c0 = (t & 3) * 16;
    float o[16];
#pragma unroll
    for (int i = 0; i < 4; ++i) {
      f32x4 s = *(const f32x4*)(red + row * 68 + c0 + 4 * i);
#pragma unroll
      for (int ww = 1; ww < 4; ++ww) s += *(const f32x4*)(red + ww * 64 * 68 + row * 68 + c0 + 4 * i);
#pragma unroll
      for (int r = 0; r < 4; ++r) o[4 * i + r] = s[r];
    }
    const u32x4 h0 = *(const u32x4*)(h + (m0 + row) * D_ + n0 + c0);
    const u32x4 h1 = *(const u32x4*)(h + (m0 + row) * D_ + n0 + c0 + 8);
    u32x4 o0, o1;
#pragma unroll
    for (int i = 0; i < 4; ++i) {
      o0[i] = pack2(bflo(h0[i]) + rbf(o[2 * i]), bfhi(h0[i]) + rbf(o[2 * i + 1]));
      o1[i] = pack2(bflo(h1[i]) + rbf(o[8 + 2 * i]), bfhi(h1[i]) + rbf(o[8 + 2 * i + 1]));
    }
    *(u32x4*)(out + (m0 + row) * D_ + n0 + c0) = o0;
    *(u32x4*)(out + (m0 + row) * D_ + n0 + c0 + 8) = o1;
  }
}

extern "C" void kernel_launch(void* const* d_in, const int* in_sizes, int n_in, void* d_out, int out_size,
                              void* d_ws, size_t ws_size, hipStream_t stream) {
  const u16* q = (const u16*)d_in[0];
  const u16* k = (const u16*)d_in[1];
  const u16* v = (const u16*)d_in[2];
  const u16* shk = (const u16*)d_in[3];
  const u16* ssmk = (const u16*)d_in[4];
  const u16* ssmD = (const u16*)d_in[5];
  const u16* wg = (const u16*)d_in[7];
  const u16* wu = (const u16*)d_in[8];
  const u16* wd = (const u16*)d_in[9];
  unsigned char* ws = (unsigned char*)d_ws;
  u16* kvR = (u16*)(ws);
  u16* xT = (u16*)(ws + (2u << 20));
  float* qm = (float*)(ws + (4u << 20));
  float* qden = qm + 2048;
  float* cs = (float*)(ws + (4u << 20) + 65536);
  u16* h = (u16*)(ws + (5u << 20));
  u16* hh = (u16*)(ws + (8u << 20));
  k1_prep<<<568, 256, 0, stream>>>(q, k, v, shk, wg, wu, kvR, xT, qm, qden, cs);
  k2_conv<<<256, 256, 0, stream>>>(ssmk, ssmD, kvR, xT, qm, qden, h);
  k3_gemm1<<<256, 256, 0, stream>>>(h, wg, wu, cs, hh);
  k4_gemm2<<<256, 256, 0, stream>>>(hh, wd, h, (u16*)d_out);
}
```

```cpp
#include <hip/hip_runtime.h>
#include <hip/hip_bf16.h>
#include <stdint.h>

typedef unsigned short u16;
typedef __attribute__((ext_vector_type(8))) short bf16x8;
typedef __attribute__((ext_vector_type(4))) short s16x4;
typedef __attribute__((ext_vector_type(16))) float f32x16;
typedef __attribute__((ext_vector_type(4))) float f32x4;
typedef __attribute__((ext_vector_type(4))) unsigned u32x4;
typedef __attribute__((ext_vector_type(2))) unsigned u32x2;
typedef __attribute__((ext_vector_type(2))) __bf16 bf16x2_t;
typedef __attribute__((ext_vector_type(4))) short v4i16_t;

#define DEV __device__ __forceinline__

static constexpr int S_ = 2048, D_ = 512, F_ = 1792;

DEV float bf2f(unsigned x) { return __uint_as_float(x << 16); }
DEV float bflo(unsigned x) { return __uint_as_float(x << 16); }
DEV float bfhi(unsigned x) { return __uint_as_float(x & 0xffff0000u); }
DEV unsigned f2bf(float x) { return (unsigned)__builtin_bit_cast(u16, (__bf16)x); }
DEV float rbf(float x) { return (float)(__bf16)x; }
DEV unsigned pack2(float lo, float hi) { return f2bf(lo) | (f2bf(hi) << 16); }

__global__ __launch_bounds__(256) void k1_prep(const u16* __restrict__ q, const u16* __restrict__ k,
                                               const u16* __restrict__ v, const u16* __restrict__ shk,
                                               const u16* __restrict__ wg, const u16* __restrict__ wu,
                                               u16* __restrict__ kvR, u16* __restrict__ xT,
                                               float* __restrict__ qm, float* __restrict__ qden,
                                               float* __restrict__ cs) {
  __shared__ __attribute__((aligned(16))) unsigned char smem[2 * 64 * 66 * 2];
  const int b = blockIdx.x, t = threadIdx.x;
  if (b < 256) {
    u16* kvs = (u16*)smem;
    u16* xs = (u16*)(smem + 64 * 66 * 2);
    const int s0 = (b & 31) * 64, d0 = (b >> 5) * 64;
    const int c = t & 7, r0 = t >> 3;
    u32x4 wv[4];
#pragma unroll
    for (int i = 0; i < 4; ++i) wv[i] = *(const u32x4*)(shk + (d0 + 8 * c) * 4 + 8 * i);
#pragma unroll
    for (int p = 0; p < 2; ++p) {
      const int r = r0 + 32 * p, s = s0 + r;
      u32x4 kr[4];
#pragma unroll
      for (int j = 0; j < 4; ++j) {
        const int ss = s - 3 + j;
        u32x4 z = {0u, 0u, 0u, 0u};
        kr[j] = (ss >= 0) ? *(const u32x4*)(k + ss * D_ + d0 + 8 * c) : z;
      }
      const u32x4 vv = *(const u32x4*)(v + s * D_ + d0 + 8 * c);
      const u32x4 qq = *(const u32x4*)(q + s * D_ + d0 + 8 * c);
#pragma unroll
      for (int e = 0; e < 8; ++e) {
        const unsigned wa = wv[e >> 1][(e & 1) * 2], wb = wv[e >> 1][(e & 1) * 2 + 1];
        const float w0 = bflo(wa), w1 = bfhi(wa), w2 = bflo(wb), w3 = bfhi(wb);
        float kf[4];
#pragma unroll
        for (int j = 0; j < 4; ++j) kf[j] = (e & 1) ? bfhi(kr[j][e >> 1]) : bflo(kr[j][e >> 1]);
        float ks = rbf(kf[0] * w3);
        ks = rbf(ks + rbf(kf[1] * w2));
        ks = rbf(ks + rbf(kf[2] * w1));
        ks = rbf(ks + rbf(kf[3] * w0));
        const float vf = (e & 1) ? bfhi(vv[e >> 1]) : bflo(vv[e >> 1]);
        const unsigned kvb = f2bf(ks * vf);
        const unsigned qb = (e & 1) ? (qq[e >> 1] >> 16) : (qq[e >> 1] & 0xffffu);
        kvs[(8 * c + e) * 66 + (63 - r)] = (u16)kvb;
        xs[(8 * c + e) * 66 + r] = (u16)qb;
      }
    }
    __syncthreads();
#pragma unroll
    for (int p = 0; p < 2; ++p) {
      const int ch = r0 + 32 * p;
      const unsigned* a = (const unsigned*)(kvs + ch * 66 + 8 * c);
      const unsigned* bq = (const unsigned*)(xs + ch * 66 + 8 * c);
      u32x4 o1 = {a[0], a[1], a[2], a[3]};
      u32x4 o2 = {bq[0], bq[1], bq[2], bq[3]};
      *(u32x4*)(kvR + (d0 + ch) * S_ + (1984 - s0) + 8 * c) = o1;
      *(u32x4*)(xT + (d0 + ch) * S_ + s0 + 8 * c) = o2;
    }
  } else if (b < 512) {
    const int lane = t & 63, w = t >> 6;
#pragma unroll
    for (int i = 0; i < 2; ++i) {
      const int row = (b - 256) * 8 + w * 2 + i;
      const u32x4 x = *(const u32x4*)(q + row * D_ + lane * 8);
      float f[8];
#pragma unroll
      for (int j = 0; j < 4; ++j) { f[2 * j] = bflo(x[j]); f[2 * j + 1] = bfhi(x[j]); }
      float sum = 0.f;
#pragma unroll
      for (int j = 0; j < 8; ++j) sum += f[j];
#pragma unroll
      for (int o = 32; o >= 1; o >>= 1) sum += __shfl_xor(sum, o);
      const float mean = sum * (1.0f / D_);
      float sq = 0.f;
#pragma unroll
      for (int j = 0; j < 8; ++j) { const float d = f[j] - mean; sq += d * d; }
#pragma unroll
      for (int o = 32; o >= 1; o >>= 1) sq += __shfl_xor(sq, o);
      const float var = rbf(sq * (1.0f / D_));
      if (lane == 0) {
        qm[row] = rbf(mean);
        qden[row] = rbf(sqrtf(rbf(var + 1e-5f)));
      }
    }
  } else {
    float* red = (float*)smem;
    const int bb = b - 512, mat = bb / 28, tile = bb % 28;
    const u16* W = mat ? wu : wg;
    const int c = t & 7, g = t >> 3;
    float acc[8];
#pragma unroll
    for (int j = 0; j < 8; ++j) acc[j] = 0.f;
#pragma unroll
    for (int i = 0; i < 16; ++i) {
      const u32x4 x = *(const u32x4*)(W + (g * 16 + i) * F_ + tile * 64 + 8 * c);
#pragma unroll
      for (int j = 0; j < 4; ++j) { acc[2 * j] += bflo(x[j]); acc[2 * j + 1] += bfhi(x[j]); }
    }
#pragma unroll
    for (int j = 0; j < 8; ++j) red[g * 64 + 8 * c + j] = acc[j];
    __syncthreads();
    if (t < 64) {
      float s = 0.f;
#pragma unroll
      for (int i = 0; i < 32; ++i) s += red[i * 64 + t];
      cs[mat * F_ + tile * 64 + t] = s;
    }
  }
}

static constexpr int K2_COPY = 4160;
static constexpr int K2_KC = 8 * K2_COPY;
static constexpr int K2_XR = 96 * 80;
static constexpr int K2_CH = K2_KC + K2_XR;

DEV void k2_write_copies(unsigned char* kc, int unit, u32x4 cur, u32x4 nxt) {
  unsigned w[8] = {cur[0], cur[1], cur[2], cur[3], nxt[0], nxt[1], nxt[2], nxt[3]};
#pragma unroll
  for (int sh = 1; sh <= 8; ++sh) {
    u32x4 o;
    const int a = sh >> 1;
    if (sh & 1) {
#pragma unroll
      for (int i = 0; i < 4; ++i) o[i] = __builtin_amdgcn_alignbit(w[a + i + 1], w[a + i], 16);
    } else {
#pragma unroll
      for (int i = 0; i < 4; ++i) o[i] = w[a + i];
    }
    *(u32x4*)(kc + (sh - 1) * K2_COPY + unit * 16) = o;
  }
}

__global__ __launch_bounds__(512) void k2_conv(const u16* __restrict__ ssmk, const u16* __restrict__ ssmD,
                                               const u16* __restrict__ kvR, const u16* __restrict__ xT,
                                               const float* __restrict__ qm, const float* __restrict__ qden,
                                               u16* __restrict__ hT) {
  __shared__ __attribute__((aligned(16))) unsigned char smem[2 * K2_CH];
  const int t = threadIdx.x, lane = t & 63, w = t >> 6;
  const int d0 = blockIdx.x * 2;
  const u32x4 z4 = {0u, 0u, 0u, 0u};
  const int ch = w >> 2, qq = w & 3;
  const int il = lane & 7, ih = (lane >> 3) & 3, hh = lane >> 5, col = lane & 31;
  const int d = d0 + ch;
  const int tv = t & 255;
  const u16* kk = ssmk + d * S_;
  const u32x4 cur = *(const u32x4*)(kk + tv * 8);
  const u32x4 nxt = (tv < 255) ? *(const u32x4*)(kk + tv * 8 + 8) : z4;
  const u32x4 xv = *(const u32x4*)(kvR + d * S_ + tv * 8);
  const int nb0 = 1024 * (qq >> 1) + 32 * col + 16 * (qq & 1) + 4 * hh;
  u32x2 xq[2];
  f32x4 mm[2], dd[2];
#pragma unroll
  for (int e = 0; e < 2; ++e) {
    xq[e] = *(const u32x2*)(xT + d * S_ + nb0 + 8 * e);
    mm[e] = *(const f32x4*)(qm + nb0 + 8 * e);
    dd[e] = *(const f32x4*)(qden + nb0 + 8 * e);
  }
  const float Dd = bf2f(ssmD[d]);
  unsigned char* base = smem + ch * K2_CH;
  *(u32x4*)(base + K2_KC + (tv >> 2) * 80 + (tv & 3) * 16) = xv;
  if (tv < 128) *(u32x4*)(base + K2_KC + (64 + (tv >> 2)) * 80 + (tv & 3) * 16) = z4;
  k2_write_copies(base, tv + 4, cur, nxt);
  if (tv < 4) {
    const u32x4 c0 = *(const u32x4*)(kk);
    k2_write_copies(base, tv, z4, tv == 3 ? c0 : z4);
  }
  __syncthreads();

  const unsigned char* kcb = base + il * K2_COPY + (2 + ih + hh + 32 * qq) * 16;
  const unsigned char* xb = base + K2_KC + (63 - col + 8 * qq) * 80 + (16 + 8 * hh) * 2;
  f32x16 acc0, acc1;
#pragma unroll
  for (int i = 0; i < 16; ++i) { acc0[i] = 0.f; acc1[i] = 0.f; }
#pragma unroll
  for (int c = 0; c < 8; ++c) {
#pragma unroll
    for (int s = 0; s < 2; ++s) {
      const bf16x8 a0 = *(const bf16x8*)(kcb + (4 * c - 2 * s) * 16);
      const bf16x8 a1 = *(const bf16x8*)(kcb + (4 * c - 2 * s + 128) * 16);
      const bf16x8 b0 = *(const bf16x8*)(xb + c * 80 - 32 * s);
      const bf16x8 b1 = *(const bf16x8*)(xb + (c - 32) * 80 - 32 * s);
      acc0 = __builtin_amdgcn_mfma_f32_32x32x16_bf16(a0, b0, acc0, 0, 0, 0);
      acc1 = __builtin_amdgcn_mfma_f32_32x32x16_bf16(a0, b1, acc1, 0, 0, 0);
      acc1 = __builtin_amdgcn_mfma_f32_32x32x16_bf16(a1, b0, acc1, 0, 0, 0);
    }
  }
  __syncthreads();
  f32x4* red = (f32x4*)base;
#pragma unroll
  for (int o = 0; o < 4; ++o) {
    if (o != qq) {
#pragma unroll
      for (int g = 0; g < 2; ++g) {
        const int r = (o & 1) * 8 + g * 4;
        f32x4 vv;
        if (o >> 1) vv = (f32x4){acc1[r], acc1[r + 1], acc1[r + 2], acc1[r + 3]};
        else vv = (f32x4){acc0[r], acc0[r + 1], acc0[r + 2], acc0[r + 3]};
        red[((o * 4 + qq) * 2 + g) * 64 + lane] = vv;
      }
    }
  }
  f32x4 y[2];
#pragma unroll
  for (int g = 0; g < 2; ++g) {
    float tmp[4];
#pragma unroll
    for (int r = 0; r < 4; ++r) {
      const float a = (qq & 1) ? acc0[8 + g * 4 + r] : acc0[g * 4 + r];
      const float b = (qq & 1) ? acc1[8 + g * 4 + r] : acc1[g * 4 + r];
      tmp[r] = (qq >> 1) ? b : a;
    }
    y[g] = (f32x4){tmp[0], tmp[1], tmp[2], tmp[3]};
  }
  __syncthreads();
#pragma unroll
  for (int sidx = 1; sidx < 4; ++sidx) {
    const int src = (qq + sidx) & 3;
#pragma unroll
    for (int g = 0; g < 2; ++g) y[g] += red[((qq * 4 + src) * 2 + g) * 64 + lane];
  }
  unsigned pk[2][2];
#pragma unroll
  for (int e = 0; e < 2; ++e) {
    const int t0 = 2044 - (nb0 + 8 * e);
    const u32x2 kvp = *(const u32x2*)(base + K2_KC + (t0 >> 5) * 80 + (t0 & 31) * 2);
    const float kvf[4] = {bfhi(kvp[1]), bflo(kvp[1]), bfhi(kvp[0]), bflo(kvp[0])};
    const float xf[4] = {bflo(xq[e][0]), bfhi(xq[e][0]), bflo(xq[e][1]), bfhi(xq[e][1])};
    float hv[4];
#pragma unroll
    for (int r = 0; r < 4; ++r) {
      const float yy = rbf(y[e][r]);
      const float y2 = rbf(yy + rbf(kvf[r] * Dd));
      const float qn = rbf(rbf(xf[r] - mm[e][r]) / dd[e][r]);
      hv[r] = xf[r] + rbf(qn * y2);
    }
    pk[e][0] = pack2(hv[0], hv[1]);
    pk[e][1] = pack2(hv[2], hv[3]);
  }
  auto s0 = __builtin_amdgcn_permlane32_swap(pk[0][0], pk[1][0], false, false);
  auto s1 = __builtin_amdgcn_permlane32_swap(pk[0][1], pk[1][1], false, false);
  const u32x4 ov = {s0[0], s1[0], s0[1], s1[1]};
  *(u32x4*)(hT + d * S_ + 1024 * (qq >> 1) + 32 * col + 16 * (qq & 1) + 8 * hh) = ov;
}

static constexpr int K3_A = 0, K3_G = 16384, K3_U = 16384 + 14336 + 128, K3_STAGE = 45184;

DEV void glds16(const void* gsrc, unsigned lds_dst) {
  unsigned keep;
  asm volatile("s_mov_b32 %0, m0\n\ts_mov_b32 m0, %2\n\ts_nop 0\n\tglobal_load_lds_dwordx4 %1, off\n\ts_mov_b32 m0, %0"
               : "=&s"(keep) : "v"(gsrc), "s"(lds_dst) : "memory");
}
#define GLDS(src, dst) glds16((const void*)(src), (unsigned)(size_t)(dst))
#define WAITV(n) asm volatile("s_waitcnt vmcnt(" #n ")" ::: "memory")

typedef __attribute__((address_space(3))) unsigned char lds_u8;

DEV s16x4 tr_read(const lds_u8* p) {
  return __builtin_bit_cast(s16x4, __builtin_amdgcn_ds_read_tr16_b64_v4i16((__attribute__((address_space(3))) v4i16_t*)p));
}
DEV bf16x8 tr_frag(const lds_u8* p, int rowstride4) {
  const s16x4 lo = tr_read(p);
  const s16x4 hi = tr_read(p + rowstride4);
  return (bf16x8){lo[0], lo[1], lo[2], lo[3], hi[0], hi[1], hi[2], hi[3]};
}

__global__ __launch_bounds__(512) void k3_gemm1(const u16* __restrict__ hT, const u16* __restrict__ wg,
                                                const u16* __restrict__ wu, const float* __restrict__ cs,
                                                u16* __restrict__ hh) {
  __shared__ __attribute__((aligned(16))) unsigned char smem_[3 * K3_STAGE + 1024];
  lds_u8* smem = (lds_u8*)smem_;
  const int t = threadIdx.x, lane = t & 63;
  const int w = __builtin_amdgcn_readfirstlane(t >> 6);
  const int w4 = w & 3, grp = w >> 2;
  const int b = blockIdx.x, x = b & 7, idx = b >> 3;
  const int mt = (x & 1) * 8 + (idx & 7), ft = (x >> 1) * 4 + (idx >> 3);
  const int m0 = mt * 128, f0 = ft * 112;

  const u16* asrc[2];
#pragma unroll
  for (int i = 0; i < 2; ++i) {
    const int p = (w * 2 + i) * 64 + lane, row = p >> 4, slot = p & 15;
    const int chunk = slot ^ ((row & 3) << 2);
    asrc[i] = hT + row * S_ + m0 + chunk * 8;
  }
  const u16* wsrc[4];
  int wdst[4];
#pragma unroll
  for (int i = 0; i < 4; ++i) {
    int j = grp ? 16 + w4 * 3 + i : w4 * 4 + i;
    if (j > 27) j = 27;
    const int mat = j >= 14, jj = j - 14 * mat;
    const int p = jj * 64 + lane, row = p / 14, part = p % 14;
    wsrc[i] = (mat ? wu : wg) + row * F_ + f0 + part * 8;
    wdst[i] = (mat ? K3_U : K3_G) + jj * 1024;
  }
  auto issue = [&](int kt, int buf) {
    lds_u8* sb = smem + buf * K3_STAGE;
#pragma unroll
    for (int i = 0; i < 2; ++i) GLDS(asrc[i] + kt * 64 * S_, sb + K3_A + (w * 2 + i) * 1024);
#pragma unroll
    for (int i = 0; i < 3; ++i) GLDS(wsrc[i] + kt * 64 * F_, sb + wdst[i]);
    if (grp == 0) GLDS(wsrc[3] + kt * 64 * F_, sb + wdst[3]);
  };
  issue(0, 0);
  issue(1, 1);
  float* csl = (float*)(smem_ + 3 * K3_STAGE);
  float csv = 0.f;
  if (t < 224) csv = cs[(t >= 112 ? F_ - 112 : 0) + f0 + t];

  f32x16 acc[7];
#pragma unroll
  for (int j = 0; j < 7; ++j)
#pragma unroll
    for (int i = 0; i < 16; ++i) acc[j][i] = 0.f;
  float sum = 0.f, sq = 0.f;
  const int hf = lane >> 5, r32 = lane & 31, q4 = (lane & 15) >> 2;
  const int boff = K3_A + (32 * grp + 8 * hf + q4) * 256 +
                   (((4 * w4 + 2 * ((lane >> 4) & 1) + ((lane & 3) >> 1)) ^ (q4 << 2)) * 16) + (lane & 1) * 8;
  const int woff = (((lane >> 4) & 1) ? K3_U : K3_G) + (32 * grp + 8 * hf + q4) * 224 + (lane & 3) * 8;
  const bf16x2_t ones = {(__bf16)1.0f, (__bf16)1.0f};

  for (int kt = 0; kt < 8; ++kt) {
    if (kt < 7) { if (grp == 0) WAITV(6); else WAITV(5); } else WAITV(0);
    __builtin_amdgcn_s_barrier();
    if (kt + 2 < 8) issue(kt + 2, (kt + 2) % 3);
    const lds_u8* sb = smem + (kt % 3) * K3_STAGE;
#pragma unroll
    for (int ks = 0; ks < 2; ++ks) {
      const bf16x8 bfrag = tr_frag(sb + boff + ks * 16 * 256, 4 * 256);
      const u32x4 bu = __builtin_bit_cast(u32x4, bfrag);
#pragma unroll
      for (int i = 0; i < 4; ++i) {
        const unsigned xi = bu[i];
        const bf16x2_t xx = __builtin_bit_cast(bf16x2_t, xi);
        sum = __builtin_amdgcn_fdot2_f32_bf16(xx, ones, sum, false);
        sq = __builtin_amdgcn_fdot2_f32_bf16(xx, xx, sq, false);
      }
#pragma unroll
      for (int j = 0; j < 7; ++j) {
        const bf16x8 afrag = tr_frag(sb + woff + ks * 16 * 224 + j * 32, 4 * 224);
        acc[j] = __builtin_amdgcn_mfma_f32_32x32x16_bf16(afrag, bfrag, acc[j], 0, 0, 0);
      }
    }
  }
  __syncthreads();
  if (t < 224) csl[t] = csv;
  sum += __shfl_xor(sum, 32);
  sq += __shfl_xor(sq, 32);
  f32x4* red = (f32x4*)smem_;
  float* sred = (float*)(smem_ + 131072);
#pragma unroll
  for (int j = 0; j < 7; ++j) {
    if ((j < 4) == (grp == 1)) {
      const int jj = j < 4 ? j : j - 4;
#pragma unroll
      for (int g = 0; g < 4; ++g)
        red[((w * 4 + jj) * 4 + g) * 64 + lane] = (f32x4){acc[j][4 * g], acc[j][4 * g + 1], acc[j][4 * g + 2], acc[j][4 * g + 3]};
    }
  }
  if (lane < 32) { sred[w * 64 + lane] = sum; sred[w * 64 + 32 + lane] = sq; }
  __syncthreads();
  const int pw = w ^ 4;
  sum += sred[pw * 64 + r32];
  sq += sred[pw * 64 + 32 + r32];
  const float mean = sum * (1.0f / D_);
  const float var = rbf(fmaxf(sq * (1.0f / D_) - mean * mean, 0.f));
  const float mb = rbf(mean);
  const float inv = 1.0f / rbf(sqrtf(rbf(var + 1e-5f)));
  u16* orow = hh + (size_t)(m0 + w4 * 32 + r32) * F_ + f0 + 8 * hf;
#pragma unroll
  for (int j = 0; j < 7; ++j) {
    if ((j < 4) == (grp == 0)) {
      const int jj = j < 4 ? j : j - 4;
      float o[8];
#pragma unroll
      for (int g = 0; g < 2; ++g) {
        const f32x4 cg = *(const f32x4*)(csl + 16 * j + 8 * g + 4 * hf);
        const f32x4 cu = *(const f32x4*)(csl + 112 + 16 * j + 8 * g + 4 * hf);
        const f32x4 pg = red[((pw * 4 + jj) * 4 + g) * 64 + lane];
        const f32x4 pu = red[((pw * 4 + jj) * 4 + 2 + g) * 64 + lane];
#pragma unroll
        for (int r = 0; r < 4; ++r) {
          const float gt = rbf((acc[j][4 * g + r] + pg[r] - mb * cg[r]) * inv);
          const float up = rbf((acc[j][8 + 4 * g + r] + pu[r] - mb * cu[r]) * inv);
          o[4 * g + r] = (gt > 0.f ? gt : 0.f) * up;
        }
      }
      unsigned a0 = pack2(o[0], o[1]), a1 = pack2(o[2], o[3]);
      unsigned b0 = pack2(o[4], o[5]), b1 = pack2(o[6], o[7]);
      auto s0 = __builtin_amdgcn_permlane32_swap(a0, b0, false, false);
      auto s1 = __builtin_amdgcn_permlane32_swap(a1, b1, false, false);
      u32x4 ov = {s0[0], s1[0], s0[1], s1[1]};
      *(u32x4*)(orow + 16 * j) = ov;
    }
  }
}

static constexpr int K4_UNIT = 8192, K4_WAVE = 5 * K4_UNIT;

__global__ __launch_bounds__(256) void k4_gemm2(const u16* __restrict__ hh, const u16* __restrict__ wd,
                                                const u16* __restrict__ hT, u16* __restrict__ out) {
  __shared__ __attribute__((aligned(16))) unsigned char smem_[4 * K4_WAVE];
  lds_u8* smem = (lds_u8*)smem_;
  const int t = threadIdx.x, lane = t & 63;
  const int w = __builtin_amdgcn_readfirstlane(t >> 6);
  const int b = blockIdx.x, x = b & 7, idx = b >> 3;
  const int mt = x * 4 + (idx & 3), nt = idx >> 2;
  const int m0 = mt * 64, n0 = nt * 64;
  lds_u8* wb = smem + w * K4_WAVE;
  const int kbase = w * 448;
  const int prow = lane >> 3, pslot = lane & 7;
  const u16* asrc = hh + (size_t)(m0 + prow) * F_ + kbase + (pslot ^ ((prow >> 1) & 7)) * 8;
  const u16* asrc2 = hh + (size_t)(m0 + prow) * F_ + kbase + (pslot ^ (((prow >> 1) + 4) & 7)) * 8;
  const u16* bsrc = wd + (size_t)(kbase + prow) * D_ + n0 + (pslot ^ (((prow >> 1) & 1) << 2)) * 8;
  auto issueA = [&](int st) {
    lds_u8* sb = wb + ((2 * st) % 5) * K4_UNIT;
#pragma unroll
    for (int i = 0; i < 8; ++i) GLDS(((i & 1) ? asrc2 : asrc) + (size_t)i * 8 * F_ + st * 64, sb + i * 1024);
  };
  auto issueB = [&](int st) {
    lds_u8* sb = wb + ((2 * st + 1) % 5) * K4_UNIT;
#pragma unroll
    for (int i = 0; i < 8; ++i) GLDS(bsrc + (size_t)(st * 64 + i * 8) * D_, sb + i * 1024);
  };
  issueA(0); issueB(0); issueA(1); issueB(1); issueA(2);
  const u32x4 hr0 = *(const u32x4*)(hT + (n0 + lane) * S_ + m0 + 16 * w);
  const u32x4 hr1 = *(const u32x4*)(hT + (n0 + lane) * S_ + m0 + 16 * w + 8);
  f32x16 acc[2][2];
#pragma unroll
  for (int i = 0; i < 2; ++i)
#pragma unroll
    for (int j = 0; j < 2; ++j)
#pragma unroll
      for (int e = 0; e < 16; ++e) acc[i][j][e] = 0.f;
  const int hf = lane >> 5, r32 = lane & 31;
  const int asw = (r32 >> 1) & 7;
  const int bq = (lane & 15) >> 2;
  const int bsw = ((bq >> 1) & 1) << 2;
  const int bcol = 16 * ((lane >> 4) & 1) + 4 * (lane & 3);
  const int boff0 = (8 * hf + bq) * 128 + (((bcol >> 3) ^ bsw) * 16) + (bcol & 7) * 2;
  const int boff1 = (8 * hf + bq) * 128 + ((((bcol + 32) >> 3) ^ bsw) * 16) + (bcol & 7) * 2;

#pragma unroll
  for (int st = 0; st < 7; ++st) {
    if (st < 5) WAITV(24); else if (st == 5) WAITV(16); else WAITV(0);
    const lds_u8* sa = wb + ((2 * st) % 5) * K4_UNIT;
    const lds_u8* sbb = wb + ((2 * st + 1) % 5) * K4_UNIT;
#pragma unroll
    for (int ks = 0; ks < 4; ++ks) {
      bf16x8 af[2], bf[2];
#pragma unroll
      for (int mb = 0; mb < 2; ++mb)
        af[mb] = *(const __attribute__((address_space(3))) bf16x8*)(sa + (r32 + 32 * mb) * 128 + (((2 * ks + hf) ^ asw) * 16));
      bf[0] = tr_frag(sbb + boff0 + ks * 16 * 128, 4 * 128);
      bf[1] = tr_frag(sbb + boff1 + ks * 16 * 128, 4 * 128);
#pragma unroll
      for (int mb = 0; mb < 2; ++mb)
#pragma unroll
        for (int nb = 0; nb < 2; ++nb)
          acc[mb][nb] = __builtin_amdgcn_mfma_f32_32x32x16_bf16(af[mb], bf[nb], acc[mb][nb], 0, 0, 0);
    }
    if (st + 2 < 7) issueB(st + 2);
    if (st + 3 < 7) issueA(st + 3);
  }
  __syncthreads();
  float* red = (float*)smem_;
#pragma unroll
  for (int mb = 0; mb < 2; ++mb)
#pragma unroll
    for (int nb = 0; nb < 2; ++nb)
#pragma unroll
      for (int e = 0; e < 16; ++e) {
        const int row = 32 * mb + (e & 3) + 8 * (e >> 2) + 4 * hf;
        red[w * 4096 + row * 64 + 32 * nb + r32] = acc[mb][nb][e];
      }
  __syncthreads();
  u16* ob = (u16*)(smem_ + 65536);
#pragma unroll
  for (int i = 0; i < 16; ++i) {
    const int row = 16 * w + i;
    const float s = (red[row * 64 + lane] + red[4096 + row * 64 + lane]) + (red[8192 + row * 64 + lane] + red[12288 + row * 64 + lane]);
    const unsigned hw = i < 8 ? hr0[(i & 7) >> 1] : hr1[(i & 7) >> 1];
    const float hv = (i & 1) ? bfhi(hw) : bflo(hw);
    ob[row * 72 + lane] = (u16)f2bf(hv + rbf(s));
  }
  __syncthreads();
#pragma unroll
  for (int i = 0; i < 2; ++i) {
    const int p = t + 256 * i, row = p >> 3, c = p & 7;
    *(u32x4*)(out + (m0 + row) * D_ + n0 + c * 8) = *(const u32x4*)(ob + row * 72 + c * 8);
  }
}

extern "C" void kernel_launch(void* const* d_in, const int* in_sizes, int n_in, void* d_out, int out_size,
                              void* d_ws, size_t ws_size, hipStream_t stream) {
  const u16* q = (const u16*)d_in[0];
  const u16* k = (const u16*)d_in[1];
  const u16* v = (const u16*)d_in[2];
  const u16* shk = (const u16*)d_in[3];
  const u16* ssmk = (const u16*)d_in[4];
  const u16* ssmD = (const u16*)d_in[5];
  const u16* wg = (const u16*)d_in[7];
  const u16* wu = (const u16*)d_in[8];
  const u16* wd = (const u16*)d_in[9];
  unsigned char* ws = (unsigned char*)d_ws;
  u16* kvR = (u16*)(ws);
  u16* xT = (u16*)(ws + (2u << 20));
  float* qm = (float*)(ws + (4u << 20));
  float* qden = qm + 2048;
  float* cs = (float*)(ws + (4u << 20) + 65536);
  u16* h = (u16*)(ws + (5u << 20));
  u16* hh = (u16*)(ws + (8u << 20));
  k1_prep<<<568, 256, 0, stream>>>(q, k, v, shk, wg, wu, kvR, xT, qm, qden, cs);
  k2_conv<<<256, 512, 0, stream>>>(ssmk, ssmD, kvR, xT, qm, qden, h);
  k3_gemm1<<<256, 512, 0, stream>>>(h, wg, wu, cs, hh);
  k4_gemm2<<<256, 256, 0, stream>>>(hh, wd, h, (u16*)d_out);
}
```

```cpp
#include <hip/hip_runtime.h>
#include <hip/hip_bf16.h>
#include <stdint.h>

typedef unsigned short u16;
typedef __attribute__((ext_vector_type(8))) short bf16x8;
typedef __attribute__((ext_vector_type(4))) short s16x4;
typedef __attribute__((ext_vector_type(16))) float f32x16;
typedef __attribute__((ext_vector_type(4))) float f32x4;
typedef __attribute__((ext_vector_type(2))) float f32x2;
typedef __attribute__((ext_vector_type(4))) unsigned u32x4;
typedef __attribute__((ext_vector_type(2))) unsigned u32x2;
typedef __attribute__((ext_vector_type(2))) __bf16 bf16x2_t;
typedef __attribute__((ext_vector_type(4))) short v4i16_t;
typedef __attribute__((address_space(3))) unsigned char lds_u8;

#define DEV __device__ __forceinline__
#define STAMP(kid,i)
#define DRAIN()

static constexpr int S_ = 2048, D_ = 512, F_ = 1792;

DEV float bflo(unsigned x) { return __uint_as_float(x << 16); }
DEV float bfhi(unsigned x) { return __uint_as_float(x & 0xffff0000u); }
DEV f32x2 unpk(unsigned x) { return (f32x2){bflo(x), bfhi(x)}; }
DEV unsigned pk2(f32x2 v) {
  bf16x2_t r = {(__bf16)v[0], (__bf16)v[1]};
  return __builtin_bit_cast(unsigned, r);
}
DEV f32x2 rbf2(f32x2 v) { return unpk(pk2(v)); }
DEV float rbf(float x) { return (float)(__bf16)x; }

DEV void glds16(const void* sbase, unsigned voff, unsigned lds_dst) {
  asm volatile("s_mov_b32 m0, %2\n\ts_nop 0\n\tglobal_load_lds_dwordx4 %0, %1"
               :: "v"(voff), "s"(sbase), "s"(lds_dst) : "memory");
}
#define WAITV(n) asm volatile("s_waitcnt vmcnt(" #n ")" ::: "memory")

DEV s16x4 tr_read(const lds_u8* p) {
  return __builtin_bit_cast(s16x4, __builtin_amdgcn_ds_read_tr16_b64_v4i16((__attribute__((address_space(3))) v4i16_t*)p));
}
DEV bf16x8 tr_frag(const lds_u8* p, int rowstride4) {
  const s16x4 lo = tr_read(p);
  const s16x4 hi = tr_read(p + rowstride4);
  return (bf16x8){lo[0], lo[1], lo[2], lo[3], hi[0], hi[1], hi[2], hi[3]};
}

__global__ __launch_bounds__(512) void k1_prep(const u16* __restrict__ q, const u16* __restrict__ k,
                                               const u16* __restrict__ v, const u16* __restrict__ shk,
                                               const u16* __restrict__ wg, const u16* __restrict__ wu,
                                               u16* __restrict__ kvR, u16* __restrict__ xT,
                                               float* __restrict__ qm, float* __restrict__ qinv,
                                               u16* __restrict__ wp, float* __restrict__ csp) {
  __shared__ __attribute__((aligned(16))) unsigned char smem[18432];
  const int b = blockIdx.x, t = threadIdx.x;
  if (b < 256) {
    u16* kvs = (u16*)smem;
    u16* xs = (u16*)(smem + 64 * 66 * 2);
    const int s0 = (b & 31) * 64, d0 = (b >> 5) * 64;
    const int c = t & 7, r = t >> 3, s = s0 + r;
    u32x4 wv[4], kr[4];
#pragma unroll
    for (int i = 0; i < 4; ++i) wv[i] = *(const u32x4*)(shk + (d0 + 8 * c) * 4 + 8 * i);
#pragma unroll
    for (int j = 0; j < 4; ++j) {
      const int ss = s - 3 + j;
      u32x4 z = {0u, 0u, 0u, 0u};
      kr[j] = (ss >= 0) ? *(const u32x4*)(k + ss * D_ + d0 + 8 * c) : z;
    }
    const u32x4 vv = *(const u32x4*)(v + s * D_ + d0 + 8 * c);
    const u32x4 qq = *(const u32x4*)(q + s * D_ + d0 + 8 * c);
#pragma unroll
    for (int p = 0; p < 4; ++p) {
      const f32x2 w0 = {bflo(wv[p][0]), bflo(wv[p][2])}, w1 = {bfhi(wv[p][0]), bfhi(wv[p][2])};
      const f32x2 w2 = {bflo(wv[p][1]), bflo(wv[p][3])}, w3 = {bfhi(wv[p][1]), bfhi(wv[p][3])};
      f32x2 ks = rbf2(unpk(kr[0][p]) * w3);
      ks = rbf2(ks + rbf2(unpk(kr[1][p]) * w2));
      ks = rbf2(ks + rbf2(unpk(kr[2][p]) * w1));
      ks = rbf2(ks + rbf2(unpk(kr[3][p]) * w0));
      const unsigned kvb = pk2(ks * unpk(vv[p]));
      kvs[(8 * c + 2 * p) * 66 + (63 - r)] = (u16)kvb;
      kvs[(8 * c + 2 * p + 1) * 66 + (63 - r)] = (u16)(kvb >> 16);
      xs[(8 * c + 2 * p) * 66 + r] = (u16)qq[p];
      xs[(8 * c + 2 * p + 1) * 66 + r] = (u16)(qq[p] >> 16);
    }
    __syncthreads();
    {
      const int ch = r;
      const unsigned* a = (const unsigned*)(kvs + ch * 66 + 8 * c);
      const unsigned* bq = (const unsigned*)(xs + ch * 66 + 8 * c);
      u32x4 o1 = {a[0], a[1], a[2], a[3]};
      u32x4 o2 = {bq[0], bq[1], bq[2], bq[3]};
      *(u32x4*)(kvR + (d0 + ch) * S_ + (1984 - s0) + 8 * c) = o1;
      *(u32x4*)(xT + (d0 + ch) * S_ + s0 + 8 * c) = o2;
    }
  } else if (b < 384) {
    const int lane = t & 63, w = t >> 6;
#pragma unroll
    for (int i = 0; i < 2; ++i) {
      const int row = (b - 256) * 16 + w * 2 + i;
      const u32x4 x = *(const u32x4*)(q + row * D_ + lane * 8);
      f32x2 f[4];
#pragma unroll
      for (int j = 0; j < 4; ++j) f[j] = unpk(x[j]);
      f32x2 s2 = (f[0] + f[1]) + (f[2] + f[3]);
      float sum = s2[0] + s2[1];
#pragma unroll
      for (int o = 32; o >= 1; o >>= 1) sum += __shfl_xor(sum, o);
      const float mean = sum * (1.0f / D_);
      f32x2 q2 = {0.f, 0.f};
#pragma unroll
      for (int j = 0; j < 4; ++j) { const f32x2 d = f[j] - mean; q2 += d * d; }
      float sq = q2[0] + q2[1];
#pragma unroll
      for (int o = 32; o >= 1; o >>= 1) sq += __shfl_xor(sq, o);
      const float var = rbf(sq * (1.0f / D_));
      if (lane == 0) {
        qm[row] = rbf(mean);
        qinv[row] = 1.0f / rbf(sqrtf(rbf(var + 1e-5f)));
      }
    }
  } else {
    float* red = (float*)smem;
    const int bb = b - 384, ft = bb & 15, kc = bb >> 4;
    const int part = t % 28, rg = t / 28;
    f32x2 acc[4] = {{0.f, 0.f}, {0.f, 0.f}, {0.f, 0.f}, {0.f, 0.f}};
    if (rg < 16) {
      const int mat = part >= 14;
      const u16* src = (mat ? wu : wg) + ft * 112 + (part - 14 * mat) * 8;
#pragma unroll
      for (int i = 0; i < 4; ++i) {
        const int kk = kc * 64 + rg * 4 + i;
        const u32x4 x = *(const u32x4*)(src + kk * F_);
        *(u32x4*)(wp + ((size_t)ft * 512 + kk) * 224 + part * 8) = x;
#pragma unroll
        for (int j = 0; j < 4; ++j) acc[j] += unpk(x[j]);
      }
      *(f32x4*)(red + rg * 224 + part * 8) = (f32x4){acc[0][0], acc[0][1], acc[1][0], acc[1][1]};
      *(f32x4*)(red + rg * 224 + part * 8 + 4) = (f32x4){acc[2][0], acc[2][1], acc[3][0], acc[3][1]};
    }
    __syncthreads();
    if (t < 224) {
      float s = 0.f;
#pragma unroll
      for (int i = 0; i < 16; ++i) s += red[i * 224 + t];
      csp[(kc * 16 + ft) * 224 + t] = s;
    }
  }
}

static constexpr int K2_COPY = 4160;
static constexpr int K2_KC = 8 * K2_COPY;
static constexpr int K2_XR = 96 * 80;
static constexpr int K2_CH = K2_KC + K2_XR;

__global__ __launch_bounds__(512) void k2_conv(const u16* __restrict__ ssmk, const u16* __restrict__ ssmD,
                                               const u16* __restrict__ kvR, const u16* __restrict__ xT,
                                               const float* __restrict__ qm, const float* __restrict__ qinv,
                                               u16* __restrict__ hT) {
  __shared__ __attribute__((aligned(16))) unsigned char smem[2 * K2_CH];
  const int t = threadIdx.x, lane = t & 63, w = t >> 6;
  const int d0 = blockIdx.x * 2;
  const u32x4 z4 = {0u, 0u, 0u, 0u};
  const int ch = w >> 2, qq = w & 3;
  const int il = lane & 7, ih = (lane >> 3) & 3, hh = lane >> 5, col = lane & 31;
  const int d = d0 + ch;
  STAMP(1,0);
  const int tv = t & 255;
  const u16* kk = ssmk + d * S_;
  u32x4 cur = *(const u32x4*)(kk + tv * 8);
  u32x4 nxt = (tv < 255) ? *(const u32x4*)(kk + tv * 8 + 8) : z4;
  const u32x4 xv = *(const u32x4*)(kvR + d * S_ + tv * 8);
  const int nb0 = 1024 * (qq >> 1) + 32 * col + 16 * (qq & 1) + 4 * hh;
  u32x2 xq[2];
  f32x4 mm[2], dd[2];
#pragma unroll
  for (int e = 0; e < 2; ++e) {
    xq[e] = *(const u32x2*)(xT + d * S_ + nb0 + 8 * e);
    mm[e] = *(const f32x4*)(qm + nb0 + 8 * e);
    dd[e] = *(const f32x4*)(qinv + nb0 + 8 * e);
  }
  const float Dd = bflo(ssmD[d]);
  unsigned char* base = smem + ch * K2_CH;
  *(u32x4*)(base + K2_KC + (tv >> 2) * 80 + (tv & 3) * 16) = xv;
  if (tv < 128) *(u32x4*)(base + K2_KC + (64 + (tv >> 2)) * 80 + (tv & 3) * 16) = z4;
  int unit = tv + 4;
#pragma unroll 1
  for (int rep = 0; rep < 2; ++rep) {
    const unsigned wd[8] = {cur[0], cur[1], cur[2], cur[3], nxt[0], nxt[1], nxt[2], nxt[3]};
#pragma unroll
    for (int sh = 1; sh <= 8; ++sh) {
      u32x4 o;
      const int a = sh >> 1;
#pragma unroll
      for (int i = 0; i < 4; ++i) o[i] = (sh & 1) ? __builtin_amdgcn_alignbit(wd[a + i + 1], wd[a + i], 16) : wd[a + i];
      *(u32x4*)(base + (sh - 1) * K2_COPY + unit * 16) = o;
    }
    if (tv >= 4) break;
    nxt = (tv == 0) ? cur : z4;
    cur = z4;
    unit = (tv + 3) & 3;
  }
  STAMP(1,1);
  __syncthreads();
  STAMP(1,2);
  const unsigned char* kcb = base + il * K2_COPY + (2 + ih + hh + 32 * qq) * 16;
  const unsigned char* xb = base + K2_KC + (63 - col + 8 * qq) * 80 + (16 + 8 * hh) * 2;
  f32x16 acc0, acc1;
#pragma unroll
  for (int i = 0; i < 16; ++i) { acc0[i] = 0.f; acc1[i] = 0.f; }
#pragma unroll 2
  for (int c = 0; c < 8; ++c) {
#pragma unroll
    for (int s = 0; s < 2; ++s) {
      const bf16x8 a0 = *(const bf16x8*)(kcb + (4 * c - 2 * s) * 16);
      const bf16x8 a1 = *(const bf16x8*)(kcb + (4 * c - 2 * s + 128) * 16);
      const bf16x8 b0 = *(const bf16x8*)(xb + c * 80 - 32 * s);
      const bf16x8 b1 = *(const bf16x8*)(xb + (c - 32) * 80 - 32 * s);
      acc0 = __builtin_amdgcn_mfma_f32_32x32x16_bf16(a0, b0, acc0, 0, 0, 0);
      acc1 = __builtin_amdgcn_mfma_f32_32x32x16_bf16(a0, b1, acc1, 0, 0, 0);
      acc1 = __builtin_amdgcn_mfma_f32_32x32x16_bf16(a1, b0, acc1, 0, 0, 0);
    }
  }
  STAMP(1,3);
  __syncthreads();
  f32x4* red = (f32x4*)base;
#pragma unroll
  for (int g = 0; g < 4; ++g) {
    red[(qq * 8 + g) * 64 + lane] = (f32x4){acc0[4 * g], acc0[4 * g + 1], acc0[4 * g + 2], acc0[4 * g + 3]};
    red[(qq * 8 + 4 + g) * 64 + lane] = (f32x4){acc1[4 * g], acc1[4 * g + 1], acc1[4 * g + 2], acc1[4 * g + 3]};
  }
  __syncthreads();
  STAMP(1,4);
  unsigned pk[2][2];
#pragma unroll
  for (int e = 0; e < 2; ++e) {
    const f32x4* rp = red + (2 * qq + e) * 64 + lane;
    const f32x4 y4 = (rp[0] + rp[512]) + (rp[1024] + rp[1536]);
    const int t0 = 2044 - (nb0 + 8 * e);
    const u32x2 kvp = *(const u32x2*)(base + K2_KC + (t0 >> 5) * 80 + (t0 & 31) * 2);
#pragma unroll
    for (int r = 0; r < 2; ++r) {
      const f32x2 kvf = {bfhi(kvp[1 - r]), bflo(kvp[1 - r])};
      const f32x2 xf = unpk(xq[e][r]);
      const f32x2 yy = rbf2((f32x2){y4[2 * r], y4[2 * r + 1]});
      const f32x2 y2 = rbf2(yy + rbf2(kvf * Dd));
      const f32x2 qn = rbf2(rbf2(xf - (f32x2){mm[e][2 * r], mm[e][2 * r + 1]}) * (f32x2){dd[e][2 * r], dd[e][2 * r + 1]});
      pk[e][r] = pk2(xf + rbf2(qn * y2));
    }
  }
  auto s0 = __builtin_amdgcn_permlane32_swap(pk[0][0], pk[1][0], false, false);
  auto s1 = __builtin_amdgcn_permlane32_swap(pk[0][1], pk[1][1], false, false);
  const u32x4 ov = {s0[0], s1[0], s0[1], s1[1]};
  STAMP(1,5);
  *(u32x4*)(hT + d * S_ + 1024 * (qq >> 1) + 32 * col + 16 * (qq & 1) + 8 * hh) = ov;
  STAMP(1,6);
  DRAIN();
  STAMP(1,7);
}

static constexpr int K3_W = 16384, K3_STAGE = 16384 + 28672;

__global__ __launch_bounds__(512) void k3_gemm1(const u16* __restrict__ hT, const u16* __restrict__ wp,
                                                const float* __restrict__ csp, u16* __restrict__ hh) {
  __shared__ __attribute__((aligned(16))) unsigned char smem_[3 * K3_STAGE + 1024];
  lds_u8* smem = (lds_u8*)smem_;
  const unsigned sbase = (unsigned)(size_t)smem;
  const int t = threadIdx.x, lane = t & 63;
  const int w = __builtin_amdgcn_readfirstlane(t >> 6);
  const int w4 = w & 3, grp = w >> 2;
  const int b = blockIdx.x, x = b & 7, idx = b >> 3;
  const int mt = (x & 1) * 8 + (idx & 7), ft = (x >> 1) * 4 + (idx >> 3);
  const int m0 = mt * 128, f0 = ft * 112;
  STAMP(2,0);
  const unsigned voffA = ((4 * w + (lane >> 4)) * S_ + (((lane & 15) ^ ((lane >> 4) << 2)) * 8)) * 2;
  const unsigned voffW = (w * 64 + lane) * 16;
  const char* abase = (const char*)(hT + m0);
  const char* wbase = (const char*)(wp + (size_t)ft * 512 * 224);
  auto issue = [&](int kt, int buf) {
    const unsigned sb = sbase + buf * K3_STAGE + w * 1024;
    const char* ap = abase + (size_t)kt * 64 * S_ * 2;
    const char* wq = wbase + kt * 28672;
    glds16(ap, voffA, sb);
    glds16(ap + 32 * S_ * 2, voffA, sb + 8192);
    glds16(wq, voffW, sb + K3_W);
    glds16(wq + 8192, voffW, sb + K3_W + 8192);
    glds16(wq + 16384, voffW, sb + K3_W + 16384);
    if (grp == 0) glds16(wq + 24576, voffW, sb + K3_W + 24576);
  };
  issue(0, 0);
  issue(1, 1);
  float* csl = (float*)(smem_ + 3 * K3_STAGE);
  if (t < 224) {
    float s = 0.f;
#pragma unroll
    for (int i = 0; i < 8; ++i) s += csp[(i * 16 + ft) * 224 + t];
    csl[t] = s;
  }

  f32x16 acc[4];
#pragma unroll
  for (int j = 0; j < 4; ++j)
#pragma unroll
    for (int i = 0; i < 16; ++i) acc[j][i] = 0.f;
  float sum = 0.f, sq = 0.f;
  const int hf = lane >> 5, r32 = lane & 31, q4 = (lane & 15) >> 2;
  const int boff = (8 * hf + q4) * 256 +
                   (((4 * w4 + 2 * ((lane >> 4) & 1) + ((lane & 3) >> 1)) ^ (q4 << 2)) * 16) + (lane & 1) * 8;
  const int woff = K3_W + (8 * hf + q4) * 448 + ((lane >> 4) & 1) * 224 + (lane & 3) * 8 + grp * 128;
  const bf16x2_t ones = {(__bf16)1.0f, (__bf16)1.0f};

  int buf = 0;
#pragma unroll 1
  for (int kt = 0; kt < 8; ++kt) {
    if (kt == 0) STAMP(2,1);
    if (kt < 7) { if (grp == 0) WAITV(6); else WAITV(5); } else WAITV(0);
    __builtin_amdgcn_s_barrier();
    if (kt == 0) STAMP(2,2);
    if (kt == 1) STAMP(2,3);
    if (kt == 4) STAMP(2,4);
    if (kt + 2 < 8) issue(kt + 2, buf == 0 ? 2 : buf - 1);
    const lds_u8* sb = smem + buf * K3_STAGE;
#pragma unroll
    for (int ks = 0; ks < 4; ++ks) {
      const bf16x8 bfrag = tr_frag(sb + boff + ks * 16 * 256, 4 * 256);
      const u32x4 bu = __builtin_bit_cast(u32x4, bfrag);
#pragma unroll
      for (int i = 0; i < 4; ++i) {
        const unsigned xi = bu[i];
        const bf16x2_t xx = __builtin_bit_cast(bf16x2_t, xi);
        sum = __builtin_amdgcn_fdot2_f32_bf16(xx, ones, sum, false);
        sq = __builtin_amdgcn_fdot2_f32_bf16(xx, xx, sq, false);
      }
#pragma unroll
      for (int j = 0; j < 3; ++j) {
        const bf16x8 afrag = tr_frag(sb + woff + ks * 16 * 448 + j * 32, 4 * 448);
        acc[j] = __builtin_amdgcn_mfma_f32_32x32x16_bf16(afrag, bfrag, acc[j], 0, 0, 0);
      }
      if (grp == 0) {
        const bf16x8 afrag = tr_frag(sb + woff + ks * 16 * 448 + 96, 4 * 448);
        acc[3] = __builtin_amdgcn_mfma_f32_32x32x16_bf16(afrag, bfrag, acc[3], 0, 0, 0);
      }
    }
    buf = buf == 2 ? 0 : buf + 1;
  }
  STAMP(2,5);
  sum += __shfl_xor(sum, 32);
  sq += __shfl_xor(sq, 32);
  const float mean = sum * (1.0f / D_);
  const float var = rbf(fmaxf(sq * (1.0f / D_) - mean * mean, 0.f));
  const float inv = 1.0f / rbf(sqrtf(rbf(var + 1e-5f)));
  const float mi = -rbf(mean) * inv;
  u16* orow = hh + (size_t)(m0 + w4 * 32 + r32) * F_ + f0 + 64 * grp + 8 * hf;
  const float* cl = csl + 64 * grp + 4 * hf;
#pragma unroll
  for (int j = 0; j < 4; ++j) {
    if (j < 3 || grp == 0) {
      unsigned o[4];
#pragma unroll
      for (int g = 0; g < 2; ++g) {
        const f32x4 cg = *(const f32x4*)(cl + 16 * j + 8 * g);
        const f32x4 cu = *(const f32x4*)(cl + 112 + 16 * j + 8 * g);
#pragma unroll
        for (int r = 0; r < 2; ++r) {
          const f32x2 ag = {acc[j][4 * g + 2 * r], acc[j][4 * g + 2 * r + 1]};
          const f32x2 au = {acc[j][8 + 4 * g + 2 * r], acc[j][8 + 4 * g + 2 * r + 1]};
          f32x2 gt = rbf2(ag * inv + (f32x2){cg[2 * r], cg[2 * r + 1]} * mi);
          const f32x2 up = rbf2(au * inv + (f32x2){cu[2 * r], cu[2 * r + 1]} * mi);
          gt[0] = gt[0] > 0.f ? gt[0] : 0.f;
          gt[1] = gt[1] > 0.f ? gt[1] : 0.f;
          o[2 * g + r] = pk2(gt * up);
        }
      }
      auto s0 = __builtin_amdgcn_permlane32_swap(o[0], o[2], false, false);
      auto s1 = __builtin_amdgcn_permlane32_swap(o[1], o[3], false, false);
      u32x4 ov = {s0[0], s1[0], s0[1], s1[1]};
      *(u32x4*)(orow + 16 * j) = ov;
    }
  }
  STAMP(2,6);
  DRAIN();
  STAMP(2,7);
}

static constexpr int K4_UNIT = 8192, K4_WAVE = 5 * K4_UNIT;

__global__ __launch_bounds__(256) void k4_gemm2(const u16* __restrict__ hh, const u16* __restrict__ wd,
                                                const u16* __restrict__ hT, u16* __restrict__ out) {
  __shared__ __attribute__((aligned(16))) unsigned char smem_[4 * K4_WAVE];
  lds_u8* smem = (lds_u8*)smem_;
  const int t = threadIdx.x, lane = t & 63;
  const int w = __builtin_amdgcn_readfirstlane(t >> 6);
  const int b = blockIdx.x, x = b & 7, idx = b >> 3;
  const int mt = x * 4 + (idx & 3), nt = idx >> 2;
  const int m0 = mt * 64, n0 = nt * 64;
  STAMP(3,0);
  lds_u8* wb = smem + w * K4_WAVE;
  const unsigned wbu = (unsigned)(size_t)wb;
  const int prow = lane >> 3, pslot = lane & 7;
  const unsigned voffA0 = (prow * F_ + (pslot ^ ((prow >> 1) & 7)) * 8) * 2;
  const unsigned voffA1 = ((prow + 8) * F_ + (pslot ^ (((prow >> 1) + 4) & 7)) * 8) * 2;
  const unsigned voffB = (prow * D_ + (pslot ^ (((prow >> 1) & 1) << 2)) * 8) * 2;
  const char* abase = (const char*)(hh + (size_t)m0 * F_ + w * 448);
  const char* bbase = (const char*)(wd + (size_t)w * 448 * D_ + n0);
  auto issueA = [&](int st, int slot) {
    const unsigned sb = wbu + slot * K4_UNIT;
    const char* p = abase + st * 128;
#pragma unroll
    for (int i = 0; i < 4; ++i) {
      glds16(p + (size_t)i * 16 * F_ * 2, voffA0, sb + i * 2048);
      glds16(p + (size_t)i * 16 * F_ * 2, voffA1, sb + i * 2048 + 1024);
    }
  };
  auto issueB = [&](int st, int slot) {
    const unsigned sb = wbu + slot * K4_UNIT;
    const char* p = bbase + (size_t)st * 64 * D_ * 2;
#pragma unroll
    for (int i = 0; i < 8; ++i) glds16(p + i * 8 * D_ * 2, voffB, sb + i * 1024);
  };
  issueA(0, 0); issueB(0, 1); issueA(1, 2); issueB(1, 3); issueA(2, 4);
  const u32x4 hr0 = *(const u32x4*)(hT + (n0 + lane) * S_ + m0 + 16 * w);
  const u32x4 hr1 = *(const u32x4*)(hT + (n0 + lane) * S_ + m0 + 16 * w + 8);
  f32x16 acc[2][2];
#pragma unroll
  for (int i = 0; i < 2; ++i)
#pragma unroll
    for (int j = 0; j < 2; ++j)
#pragma unroll
      for (int e = 0; e < 16; ++e) acc[i][j][e] = 0.f;
  const int hf = lane >> 5, r32 = lane & 31;
  const int asw = (r32 >> 1) & 7;
  const int bq = (lane & 15) >> 2;
  const int bsw = ((bq >> 1) & 1) << 2;
  const int bcol = 16 * ((lane >> 4) & 1) + 4 * (lane & 3);
  const int boff0 = (8 * hf + bq) * 128 + (((bcol >> 3) ^ bsw) * 16) + (bcol & 7) * 2;
  const int boff1 = (8 * hf + bq) * 128 + ((((bcol + 32) >> 3) ^ bsw) * 16) + (bcol & 7) * 2;

  int sa = 0;
#pragma unroll 1
  for (int st = 0; st < 7; ++st) {
    if (st == 0) STAMP(3,1);
    if (st < 5) WAITV(24); else if (st == 5) WAITV(16); else WAITV(0);
    if (st == 0) STAMP(3,2);
    if (st == 1) STAMP(3,3);
    if (st == 4) STAMP(3,4);
    const int sbt = sa == 4 ? 0 : sa + 1;
    const lds_u8* pa = wb + sa * K4_UNIT;
    const lds_u8* pb = wb + sbt * K4_UNIT;
#pragma unroll
    for (int ks = 0; ks < 4; ++ks) {
      bf16x8 af[2], bf[2];
#pragma unroll
      for (int mb = 0; mb < 2; ++mb)
        af[mb] = *(const __attribute__((address_space(3))) bf16x8*)(pa + (r32 + 32 * mb) * 128 + (((2 * ks + hf) ^ asw) * 16));
      bf[0] = tr_frag(pb + boff0 + ks * 16 * 128, 4 * 128);
      bf[1] = tr_frag(pb + boff1 + ks * 16 * 128, 4 * 128);
#pragma unroll
      for (int mb = 0; mb < 2; ++mb)
#pragma unroll
        for (int nb = 0; nb < 2; ++nb)
          acc[mb][nb] = __builtin_amdgcn_mfma_f32_32x32x16_bf16(af[mb], bf[nb], acc[mb][nb], 0, 0, 0);
    }
    if (st + 2 < 7) issueB(st + 2, sa);
    if (st + 3 < 7) issueA(st + 3, sbt);
    sa = sbt == 4 ? 0 : sbt + 1;
  }
  STAMP(3,5);
  __syncthreads();
  float* red = (float*)smem_;
#pragma unroll
  for (int mb = 0; mb < 2; ++mb)
#pragma unroll
    for (int nb = 0; nb < 2; ++nb)
#pragma unroll
      for (int g = 0; g < 4; ++g)
        *(f32x4*)(red + w * 4352 + (32 * nb + r32) * 68 + 32 * mb + 8 * g + 4 * hf) =
            (f32x4){acc[mb][nb][4 * g], acc[mb][nb][4 * g + 1], acc[mb][nb][4 * g + 2], acc[mb][nb][4 * g + 3]};
  __syncthreads();
  u16* ob = (u16*)(smem_ + 71680);
#pragma unroll
  for (int i = 0; i < 4; ++i) {
    const float* rp = red + lane * 68 + 16 * w + 4 * i;
    const f32x4 s = (*(const f32x4*)(rp) + *(const f32x4*)(rp + 4352)) + (*(const f32x4*)(rp + 8704) + *(const f32x4*)(rp + 13056));
    const unsigned h0 = i < 2 ? hr0[2 * i] : hr1[2 * i - 4];
    const unsigned h1 = i < 2 ? hr0[2 * i + 1] : hr1[2 * i - 3];
    const unsigned o0 = pk2(unpk(h0) + rbf2((f32x2){s[0], s[1]}));
    const unsigned o1 = pk2(unpk(h1) + rbf2((f32x2){s[2], s[3]}));
    u16* op = ob + (16 * w + 4 * i) * 72 + lane;
    op[0] = (u16)o0; op[72] = (u16)(o0 >> 16); op[144] = (u16)o1; op[216] = (u16)(o1 >> 16);
  }
  __syncthreads();
#pragma unroll
  for (int i = 0; i < 2; ++i) {
    const int p = t + 256 * i, row = p >> 3, c = p & 7;
    *(u32x4*)(out + (m0 + row) * D_ + n0 + c * 8) = *(const u32x4*)(ob + row * 72 + c * 8);
  }
  STAMP(3,6);
  DRAIN();
  STAMP(3,7);
}

extern "C" void kernel_launch(void* const* d_in, const int* in_sizes, int n_in, void* d_out, int out_size,
                              void* d_ws, size_t ws_size, hipStream_t stream) {
  const u16* q = (const u16*)d_in[0];
  const u16* k = (const u16*)d_in[1];
  const u16* v = (const u16*)d_in[2];
  const u16* shk = (const u16*)d_in[3];
  const u16* ssmk = (const u16*)d_in[4];
  const u16* ssmD = (const u16*)d_in[5];
  const u16* wg = (const u16*)d_in[7];
  const u16* wu = (const u16*)d_in[8];
  const u16* wd = (const u16*)d_in[9];
  unsigned char* ws = (unsigned char*)d_ws;
  u16* kvR = (u16*)(ws);
  u16* xT = (u16*)(ws + (2u << 20));
  float* qm = (float*)(ws + (4u << 20));
  float* qinv = qm + 2048;
  float* csp = (float*)(ws + (4u << 20) + 65536);
  u16* h = (u16*)(ws + (5u << 20));
  u16* hh = (u16*)(ws + (8u << 20));
  u16* wp = (u16*)(ws + (16u << 20));
  k1_prep<<<512, 512, 0, stream>>>(q, k, v, shk, wg, wu, kvR, xT, qm, qinv, wp, csp);
  k2_conv<<<256, 512, 0, stream>>>(ssmk, ssmD, kvR, xT, qm, qinv, h);
  k3_gemm1<<<256, 512, 0, stream>>>(h, wp, csp, hh);
  k4_gemm2<<<256, 256, 0, stream>>>(hh, wd, h, (u16*)d_out);
}
```

```cpp
#include <hip/hip_runtime.h>
#include <hip/hip_bf16.h>
#include <stdint.h>

typedef unsigned short u16;
typedef __attribute__((ext_vector_type(8))) short bf16x8;
typedef __attribute__((ext_vector_type(4))) short s16x4;
typedef __attribute__((ext_vector_type(16))) float f32x16;
typedef __attribute__((ext_vector_type(4))) float f32x4;
typedef __attribute__((ext_vector_type(2))) float f32x2;
typedef __attribute__((ext_vector_type(4))) unsigned u32x4;
typedef __attribute__((ext_vector_type(2))) unsigned u32x2;
typedef __attribute__((ext_vector_type(2))) __bf16 bf16x2_t;
typedef __attribute__((ext_vector_type(4))) short v4i16_t;
typedef __attribute__((address_space(3))) unsigned char lds_u8;

#define DEV __device__ __forceinline__
#define STAMP(kid,i)
#define DRAIN()

static constexpr int S_ = 2048, D_ = 512, F_ = 1792;

DEV float bflo(unsigned x) { return __uint_as_float(x << 16); }
DEV float bfhi(unsigned x) { return __uint_as_float(x & 0xffff0000u); }
DEV f32x2 unpk(unsigned x) { return (f32x2){bflo(x), bfhi(x)}; }
DEV unsigned pk2(f32x2 v) {
  bf16x2_t r = {(__bf16)v[0], (__bf16)v[1]};
  return __builtin_bit_cast(unsigned, r);
}
DEV f32x2 rbf2(f32x2 v) { return unpk(pk2(v)); }
DEV float rbf(float x) { return (float)(__bf16)x; }

DEV void glds16(const void* sbase, unsigned voff, unsigned lds_dst) {
  asm volatile("s_mov_b32 m0, %2\n\ts_nop 0\n\tglobal_load_lds_dwordx4 %0, %1"
               :: "v"(voff), "s"(sbase), "s"(lds_dst) : "memory");
}
#define WAITV(n) asm volatile("s_waitcnt vmcnt(" #n ")" ::: "memory")

DEV s16x4 tr_read(const lds_u8* p) {
  return __builtin_bit_cast(s16x4, __builtin_amdgcn_ds_read_tr16_b64_v4i16((__attribute__((address_space(3))) v4i16_t*)p));
}
DEV bf16x8 tr_frag(const lds_u8* p, int rowstride4) {
  const s16x4 lo = tr_read(p);
  const s16x4 hi = tr_read(p + rowstride4);
  return (bf16x8){lo[0], lo[1], lo[2], lo[3], hi[0], hi[1], hi[2], hi[3]};
}

__global__ __launch_bounds__(512) void k1_prep(const u16* __restrict__ q, const u16* __restrict__ k,
                                               const u16* __restrict__ v, const u16* __restrict__ shk,
                                               const u16* __restrict__ wg, const u16* __restrict__ wu,
                                               u16* __restrict__ kvR, u16* __restrict__ xT,
                                               float* __restrict__ qm, float* __restrict__ qinv,
                                               u16* __restrict__ wp, float* __restrict__ csp) {
  __shared__ __attribute__((aligned(16))) unsigned char smem[28672 + 14336];
  const int b = blockIdx.x, t = threadIdx.x;
  if (b < 256) {
    u16* kvs = (u16*)smem;
    u16* xs = (u16*)(smem + 64 * 66 * 2);
    const int s0 = (b & 31) * 64, d0 = (b >> 5) * 64;
    const int c = t & 7, r = t >> 3, s = s0 + r;
    u32x4 wv[4], kr[4];
#pragma unroll
    for (int i = 0; i < 4; ++i) wv[i] = *(const u32x4*)(shk + (d0 + 8 * c) * 4 + 8 * i);
#pragma unroll
    for (int j = 0; j < 4; ++j) {
      const int ss = s - 3 + j;
      u32x4 z = {0u, 0u, 0u, 0u};
      kr[j] = (ss >= 0) ? *(const u32x4*)(k + ss * D_ + d0 + 8 * c) : z;
    }
    const u32x4 vv = *(const u32x4*)(v + s * D_ + d0 + 8 * c);
    const u32x4 qq = *(const u32x4*)(q + s * D_ + d0 + 8 * c);
#pragma unroll
    for (int p = 0; p < 4; ++p) {
      const f32x2 w0 = {bflo(wv[p][0]), bflo(wv[p][2])}, w1 = {bfhi(wv[p][0]), bfhi(wv[p][2])};
      const f32x2 w2 = {bflo(wv[p][1]), bflo(wv[p][3])}, w3 = {bfhi(wv[p][1]), bfhi(wv[p][3])};
      f32x2 ks = rbf2(unpk(kr[0][p]) * w3);
      ks = rbf2(ks + rbf2(unpk(kr[1][p]) * w2));
      ks = rbf2(ks + rbf2(unpk(kr[2][p]) * w1));
      ks = rbf2(ks + rbf2(unpk(kr[3][p]) * w0));
      const unsigned kvb = pk2(ks * unpk(vv[p]));
      kvs[(8 * c + 2 * p) * 66 + (63 - r)] = (u16)kvb;
      kvs[(8 * c + 2 * p + 1) * 66 + (63 - r)] = (u16)(kvb >> 16);
      xs[(8 * c + 2 * p) * 66 + r] = (u16)qq[p];
      xs[(8 * c + 2 * p + 1) * 66 + r] = (u16)(qq[p] >> 16);
    }
    __syncthreads();
    {
      const int ch = r;
      const unsigned* a = (const unsigned*)(kvs + ch * 66 + 8 * c);
      const unsigned* bq = (const unsigned*)(xs + ch * 66 + 8 * c);
      u32x4 o1 = {a[0], a[1], a[2], a[3]};
      u32x4 o2 = {bq[0], bq[1], bq[2], bq[3]};
      *(u32x4*)(kvR + (d0 + ch) * S_ + (1984 - s0) + 8 * c) = o1;
      *(u32x4*)(xT + (d0 + ch) * S_ + s0 + 8 * c) = o2;
    }
  } else if (b < 384) {
    const int lane = t & 63, w = t >> 6;
#pragma unroll
    for (int i = 0; i < 2; ++i) {
      const int row = (b - 256) * 16 + w * 2 + i;
      const u32x4 x = *(const u32x4*)(q + row * D_ + lane * 8);
      f32x2 f[4];
#pragma unroll
      for (int j = 0; j < 4; ++j) f[j] = unpk(x[j]);
      f32x2 s2 = (f[0] + f[1]) + (f[2] + f[3]);
      float sum = s2[0] + s2[1];
#pragma unroll
      for (int o = 32; o >= 1; o >>= 1) sum += __shfl_xor(sum, o);
      const float mean = sum * (1.0f / D_);
      f32x2 q2 = {0.f, 0.f};
#pragma unroll
      for (int j = 0; j < 4; ++j) { const f32x2 d = f[j] - mean; q2 += d * d; }
      float sq = q2[0] + q2[1];
#pragma unroll
      for (int o = 32; o >= 1; o >>= 1) sq += __shfl_xor(sq, o);
      const float var = rbf(sq * (1.0f / D_));
      if (lane == 0) {
        qm[row] = rbf(mean);
        qinv[row] = 1.0f / rbf(sqrtf(rbf(var + 1e-5f)));
      }
    }
  } else {
    lds_u8* tile = (lds_u8*)smem;
    float* red = (float*)(smem + 28672);
    const int bb = b - 384, ft = bb & 15, kc = bb >> 4;
    const int part = t % 28, rg = t / 28;
    if (rg < 16) {
      f32x2 acc[4] = {{0.f, 0.f}, {0.f, 0.f}, {0.f, 0.f}, {0.f, 0.f}};
      const int mat = part >= 14;
      const u16* src = (mat ? wu : wg) + ft * 112 + (part - 14 * mat) * 8;
#pragma unroll
      for (int i = 0; i < 4; ++i) {
        const int kk = rg * 4 + i;
        const u32x4 x = *(const u32x4*)(src + (kc * 64 + kk) * F_);
        *(u32x4*)(smem + kk * 448 + part * 16) = x;
#pragma unroll
        for (int j = 0; j < 4; ++j) acc[j] += unpk(x[j]);
      }
      *(f32x4*)(red + rg * 224 + part * 8) = (f32x4){acc[0][0], acc[0][1], acc[1][0], acc[1][1]};
      *(f32x4*)(red + rg * 224 + part * 8 + 4) = (f32x4){acc[2][0], acc[2][1], acc[3][0], acc[3][1]};
    }
    __syncthreads();
    const int lane = t & 63, w = t >> 6;
    const int woff = (8 * (lane >> 5) + ((lane & 15) >> 2)) * 448 + ((lane >> 4) & 1) * 224 + (lane & 3) * 8;
#pragma unroll 1
    for (int f = w; f < 28; f += 8) {
      const int j = f >> 2, ks = f & 3;
      const bf16x8 fr = tr_frag(tile + woff + ks * 16 * 448 + j * 32, 4 * 448);
      *(bf16x8*)(wp + ((((size_t)ft * 7 + j) * 32 + kc * 4 + ks) * 64 + lane) * 8) = fr;
    }
    if (t < 224) {
      float s = 0.f;
#pragma unroll
      for (int i = 0; i < 16; ++i) s += red[i * 224 + t];
      csp[(kc * 16 + ft) * 224 + t] = s;
    }
  }
}

static constexpr int K2_COPY = 4160;
static constexpr int K2_KC = 8 * K2_COPY;
static constexpr int K2_XR = 96 * 80;
static constexpr int K2_CH = K2_KC + K2_XR;

__global__ __launch_bounds__(512) void k2_conv(const u16* __restrict__ ssmk, const u16* __restrict__ ssmD,
                                               const u16* __restrict__ kvR, const u16* __restrict__ xT,
                                               const float* __restrict__ qm, const float* __restrict__ qinv,
                                               u16* __restrict__ hT) {
  __shared__ __attribute__((aligned(16))) unsigned char smem[2 * K2_CH];
  const int t = threadIdx.x, lane = t & 63, w = t >> 6;
  const int d0 = blockIdx.x * 2;
  const u32x4 z4 = {0u, 0u, 0u, 0u};
  const int ch = w >> 2, qq = w & 3;
  const int il = lane & 7, ih = (lane >> 3) & 3, hh = lane >> 5, col = lane & 31;
  const int d = d0 + ch;
  STAMP(1,0);
  const int tv = t & 255;
  const u16* kk = ssmk + d * S_;
  u32x4 cur = *(const u32x4*)(kk + tv * 8);
  u32x4 nxt = (tv < 255) ? *(const u32x4*)(kk + tv * 8 + 8) : z4;
  const u32x4 xv = *(const u32x4*)(kvR + d * S_ + tv * 8);
  const int nb0 = 1024 * (qq >> 1) + 32 * col + 16 * (qq & 1) + 4 * hh;
  u32x2 xq[2];
  f32x4 mm[2], dd[2];
#pragma unroll
  for (int e = 0; e < 2; ++e) {
    xq[e] = *(const u32x2*)(xT + d * S_ + nb0 + 8 * e);
    mm[e] = *(const f32x4*)(qm + nb0 + 8 * e);
    dd[e] = *(const f32x4*)(qinv + nb0 + 8 * e);
  }
  const float Dd = bflo(ssmD[d]);
  unsigned char* base = smem + ch * K2_CH;
  *(u32x4*)(base + K2_KC + (tv >> 2) * 80 + (tv & 3) * 16) = xv;
  if (tv < 128) *(u32x4*)(base + K2_KC + (64 + (tv >> 2)) * 80 + (tv & 3) * 16) = z4;
  int unit = tv + 4;
#pragma unroll 1
  for (int rep = 0; rep < 2; ++rep) {
    const unsigned wd[8] = {cur[0], cur[1], cur[2], cur[3], nxt[0], nxt[1], nxt[2], nxt[3]};
#pragma unroll
    for (int sh = 1; sh <= 8; ++sh) {
      u32x4 o;
      const int a = sh >> 1;
#pragma unroll
      for (int i = 0; i < 4; ++i) o[i] = (sh & 1) ? __builtin_amdgcn_alignbit(wd[a + i + 1], wd[a + i], 16) : wd[a + i];
      *(u32x4*)(base + (sh - 1) * K2_COPY + unit * 16) = o;
    }
    if (tv >= 4) break;
    nxt = (tv == 0) ? cur : z4;
    cur = z4;
    unit = (tv + 3) & 3;
  }
  STAMP(1,1);
  __syncthreads();
  STAMP(1,2);
  const unsigned char* kcb = base + il * K2_COPY + (2 + ih + hh + 32 * qq) * 16;
  const unsigned char* xb = base + K2_KC + (63 - col + 8 * qq) * 80 + (16 + 8 * hh) * 2;
  f32x16 acc0, acc1;
#pragma unroll
  for (int i = 0; i < 16; ++i) { acc0[i] = 0.f; acc1[i] = 0.f; }
#pragma unroll 2
  for (int c = 0; c < 8; ++c) {
#pragma unroll
    for (int s = 0; s < 2; ++s) {
      const bf16x8 a0 = *(const bf16x8*)(kcb + (4 * c - 2 * s) * 16);
      const bf16x8 a1 = *(const bf16x8*)(kcb + (4 * c - 2 * s + 128) * 16);
      const bf16x8 b0 = *(const bf16x8*)(xb + c * 80 - 32 * s);
      const bf16x8 b1 = *(const bf16x8*)(xb + (c - 32) * 80 - 32 * s);
      acc0 = __builtin_amdgcn_mfma_f32_32x32x16_bf16(a0, b0, acc0, 0, 0, 0);
      acc1 = __builtin_amdgcn_mfma_f32_32x32x16_bf16(a0, b1, acc1, 0, 0, 0);
      acc1 = __builtin_amdgcn_mfma_f32_32x32x16_bf16(a1, b0, acc1, 0, 0, 0);
    }
  }
  STAMP(1,3);
  __syncthreads();
  f32x4* red = (f32x4*)base;
#pragma unroll
  for (int g = 0; g < 4; ++g) {
    red[(qq * 8 + g) * 64 + lane] = (f32x4){acc0[4 * g], acc0[4 * g + 1], acc0[4 * g + 2], acc0[4 * g + 3]};
    red[(qq * 8 + 4 + g) * 64 + lane] = (f32x4){acc1[4 * g], acc1[4 * g + 1], acc1[4 * g + 2], acc1[4 * g + 3]};
  }
  __syncthreads();
  STAMP(1,4);
  unsigned pk[2][2];
#pragma unroll
  for (int e = 0; e < 2; ++e) {
    const f32x4* rp = red + (2 * qq + e) * 64 + lane;
    const f32x4 y4 = (rp[0] + rp[512]) + (rp[1024] + rp[1536]);
    const int t0 = 2044 - (nb0 + 8 * e);
    const u32x2 kvp = *(const u32x2*)(base + K2_KC + (t0 >> 5) * 80 + (t0 & 31) * 2);
#pragma unroll
    for (int r = 0; r < 2; ++r) {
      const f32x2 kvf = {bfhi(kvp[1 - r]), bflo(kvp[1 - r])};
      const f32x2 xf = unpk(xq[e][r]);
      const f32x2 yy = rbf2((f32x2){y4[2 * r], y4[2 * r + 1]});
      const f32x2 y2 = rbf2(yy + rbf2(kvf * Dd));
      const f32x2 qn = rbf2(rbf2(xf - (f32x2){mm[e][2 * r], mm[e][2 * r + 1]}) * (f32x2){dd[e][2 * r], dd[e][2 * r + 1]});
      pk[e][r] = pk2(xf + rbf2(qn * y2));
    }
  }
  auto s0 = __builtin_amdgcn_permlane32_swap(pk[0][0], pk[1][0], false, false);
  auto s1 = __builtin_amdgcn_permlane32_swap(pk[0][1], pk[1][1], false, false);
  const u32x4 ov = {s0[0], s1[0], s0[1], s1[1]};
  STAMP(1,5);
  *(u32x4*)(hT + d * S_ + 1024 * (qq >> 1) + 32 * col + 16 * (qq & 1) + 8 * hh) = ov;
  STAMP(1,6);
  DRAIN();
  STAMP(1,7);
}

DEV void gload16(bf16x8& dst, const void* sbase, unsigned voff) {
  asm volatile("global_load_dwordx4 %0, %1, %2" : "=v"(dst) : "v"(voff), "s"(sbase) : "memory");
}

template <bool STATS>
DEV void k3_main(lds_u8* smem, const u16* wp, int ft, int w, int lane, float* stl, f32x16* acc) {
  const int hf = lane >> 5, q4 = (lane & 15) >> 2;
  {
    const int bsl = 2 * ((lane >> 4) & 1) + ((lane & 3) >> 1);
    int boff[4];
#pragma unroll
    for (int i = 0; i < 4; ++i)
      boff[i] = (8 * hf + q4) * 256 + (((4 * ((i + w) & 3) + bsl) ^ (q4 << 2)) * 16) + (lane & 1) * 8;
    const char* wbase = (const char*)(wp + ((size_t)ft * 7 + w) * 32 * 512);
    const unsigned voffW = lane * 16;
    bf16x8 wf[32];
#pragma unroll
    for (int i = 0; i < 12; ++i) gload16(wf[i], wbase + i * 1024, voffW);
#pragma unroll
    for (int j = 0; j < 4; ++j)
#pragma unroll
      for (int i = 0; i < 16; ++i) acc[j][i] = 0.f;
    float sum0 = 0.f, sum1 = 0.f, sq0 = 0.f, sq1 = 0.f;
    bf16x8 b0[4], b1[4];
    auto ld = [&](int kg, bf16x8* bb) {
#pragma unroll
      for (int i = 0; i < 4; ++i) bb[i] = tr_frag(smem + kg * 4096 + boff[i], 1024);
    };
    auto mm = [&](int kg, const bf16x8* bb) {
#pragma unroll
      for (int i = 0; i < 4; ++i) acc[i] = __builtin_amdgcn_mfma_f32_32x32x16_bf16(wf[kg], bb[i], acc[i], 0, 0, 0);
      if (STATS) {
        const u32x4 bu = __builtin_bit_cast(u32x4, bb[0]);
#pragma unroll
        for (int i = 0; i < 4; ++i) {
          const unsigned xi = bu[i];
          const float lo = bflo(xi), hi = bfhi(xi);
          sum0 += lo; sum1 += hi;
          sq0 = fmaf(lo, lo, sq0); sq1 = fmaf(hi, hi, sq1);
        }
        asm volatile("" : "+v"(sum0), "+v"(sum1), "+v"(sq0), "+v"(sq1));
      }
    };
    STAMP(2,1);
    asm volatile("s_waitcnt vmcnt(8)" : "+v"(wf[0]), "+v"(wf[1]), "+v"(wf[2]), "+v"(wf[3]) :: "memory");
    __builtin_amdgcn_s_barrier();
    STAMP(2,2);
    ld(0, b0);
#pragma unroll
    for (int c = 0; c < 8; ++c) {
      if (c < 5) {
#pragma unroll
        for (int i = 0; i < 4; ++i) gload16(wf[4 * c + 12 + i], wbase + (4 * c + 12 + i) * 1024, voffW);
      }
      ld(4 * c + 1, b1); mm(4 * c, b0);
      ld(4 * c + 2, b0); mm(4 * c + 1, b1);
      ld(4 * c + 3, b1); mm(4 * c + 2, b0);
      if (c < 7) {
        asm volatile("s_waitcnt vmcnt(%4)" : "+v"(wf[4 * c + 4]), "+v"(wf[4 * c + 5]), "+v"(wf[4 * c + 6]), "+v"(wf[4 * c + 7])
                     : "i"(c < 5 ? 8 : (c == 5 ? 4 : 0)) : "memory");
        __builtin_amdgcn_s_barrier();
        if (c == 0) STAMP(2,3);
        if (c == 3) STAMP(2,4);
        ld(4 * c + 4, b0);
      }
      mm(4 * c + 3, b1);
    }
    STAMP(2,5);
    if (STATS) {
      float sum = sum0 + sum1, sq = sq0 + sq1;
      sum += __shfl_xor(sum, 32);
      sq += __shfl_xor(sq, 32);
      const float mean = sum * (1.0f / D_);
      const float var = rbf(fmaxf(sq * (1.0f / D_) - mean * mean, 0.f));
      const float inv_ = 1.0f / rbf(sqrtf(rbf(var + 1e-5f)));
      if (lane < 32) *(f32x2*)(stl + (w * 32 + lane) * 2) = (f32x2){inv_, -rbf(mean) * inv_};
    }
  }
}

__global__ __launch_bounds__(512) void k3_gemm1(const u16* __restrict__ hT, const u16* __restrict__ wp,
                                                const float* __restrict__ csp, u16* __restrict__ hh) {
  __shared__ __attribute__((aligned(16))) unsigned char smem_[131072 + 2048 + 7168];
  lds_u8* smem = (lds_u8*)smem_;
  const unsigned sbase = (unsigned)(size_t)smem;
  const int t = threadIdx.x, lane = t & 63;
  const int w = __builtin_amdgcn_readfirstlane(t >> 6);
  const int b = blockIdx.x, x = b & 7, idx = b >> 3;
  const int mt = (x & 1) * 8 + (idx & 7), ft = (x >> 1) * 4 + (idx >> 3);
  const int m0 = mt * 128, f0 = ft * 112;
  STAMP(2,0);
  float* csl = (float*)(smem_ + 131072);
  float* stl = csl + 224;
  const int hf = lane >> 5;
  f32x16 acc[4];
  if (w < 4) {
    k3_main<true>(smem, wp, ft, w, lane, stl, acc);
  } else if (w < 7) {
    k3_main<false>(smem, wp, ft, w, lane, stl, acc);
  } else {
    const unsigned voffA = ((lane >> 4) * S_ + (((lane & 15) ^ ((lane >> 4) << 2)) * 8)) * 2;
    const char* abase = (const char*)(hT + m0);
    auto issue = [&](int c) {
#pragma unroll
      for (int i = 0; i < 16; ++i) glds16(abase + (size_t)(c * 64 + 4 * i) * S_ * 2, voffA, sbase + c * 16384 + i * 1024);
    };
    issue(0); issue(1); issue(2);
#pragma unroll 1
    for (int c = 0; c < 8; ++c) {
      if (c < 6) WAITV(32); else if (c == 6) WAITV(16); else WAITV(0);
      __builtin_amdgcn_s_barrier();
      if (c < 5) issue(c + 3);
    }
#pragma unroll 1
    for (int i = lane; i < 224; i += 64) {
      float s = 0.f;
#pragma unroll
      for (int k8 = 0; k8 < 8; ++k8) s += csp[(k8 * 16 + ft) * 224 + i];
      csl[i] = s;
    }
  }
  __syncthreads();
  if (w < 7) {
    f32x4 cg[2], cu[2];
#pragma unroll
    for (int g = 0; g < 2; ++g) {
      cg[g] = *(const f32x4*)(csl + 16 * w + 8 * g + 4 * hf);
      cu[g] = *(const f32x4*)(csl + 112 + 16 * w + 8 * g + 4 * hf);
    }
    u16* orow = hh + (size_t)(m0 + (lane & 31)) * F_ + f0 + 16 * w + 8 * hf;
#pragma unroll
    for (int i = 0; i < 4; ++i) {
      const int mb = (i + w) & 3;
      const f32x2 st2 = *(const f32x2*)(stl + (mb * 32 + (lane & 31)) * 2);
      const float inv = st2[0], mi = st2[1];
      unsigned o[4];
#pragma unroll
      for (int g = 0; g < 2; ++g) {
#pragma unroll
        for (int r = 0; r < 2; ++r) {
          const f32x2 ag = {acc[i][4 * g + 2 * r], acc[i][4 * g + 2 * r + 1]};
          const f32x2 au = {acc[i][8 + 4 * g + 2 * r], acc[i][8 + 4 * g + 2 * r + 1]};
          f32x2 gt = ag * inv + (f32x2){cg[g][2 * r], cg[g][2 * r + 1]} * mi;
          const f32x2 up = au * inv + (f32x2){cu[g][2 * r], cu[g][2 * r + 1]} * mi;
          gt[0] = gt[0] > 0.f ? gt[0] : 0.f;
          gt[1] = gt[1] > 0.f ? gt[1] : 0.f;
          o[2 * g + r] = pk2(gt * up);
        }
      }
      auto s0 = __builtin_amdgcn_permlane32_swap(o[0], o[2], false, false);
      auto s1 = __builtin_amdgcn_permlane32_swap(o[1], o[3], false, false);
      u32x4 ov = {s0[0], s1[0], s0[1], s1[1]};
      *(u32x4*)(orow + (size_t)mb * 32 * F_) = ov;
    }
  }
  STAMP(2,6);
  DRAIN();
  STAMP(2,7);
}

static constexpr int K4_UNIT = 8192, K4_WAVE = 5 * K4_UNIT;

__global__ __launch_bounds__(256) void k4_gemm2(const u16* __restrict__ hh, const u16* __restrict__ wd,
                                                const u16* __restrict__ hT, u16* __restrict__ out) {
  __shared__ __attribute__((aligned(16))) unsigned char smem_[4 * K4_WAVE];
  lds_u8* smem = (lds_u8*)smem_;
  const int t = threadIdx.x, lane = t & 63;
  const int w = __builtin_amdgcn_readfirstlane(t >> 6);
  const int b = blockIdx.x, x = b & 7, idx = b >> 3;
  const int mt = x * 4 + (idx & 3), nt = idx >> 2;
  const int m0 = mt * 64, n0 = nt * 64;
  STAMP(3,0);
  lds_u8* wb = smem + w * K4_WAVE;
  const unsigned wbu = (unsigned)(size_t)wb;
  const int prow = lane >> 3, pslot = lane & 7;
  const unsigned voffA0 = (prow * F_ + (pslot ^ ((prow >> 1) & 7)) * 8) * 2;
  const unsigned voffA1 = ((prow + 8) * F_ + (pslot ^ (((prow >> 1) + 4) & 7)) * 8) * 2;
  const unsigned voffB = (prow * D_ + (pslot ^ (((prow >> 1) & 1) << 2)) * 8) * 2;
  const char* abase = (const char*)(hh + (size_t)m0 * F_ + w * 448);
  const char* bbase = (const char*)(wd + (size_t)w * 448 * D_ + n0);
  auto issueA = [&](int st, int slot) {
    const unsigned sb = wbu + slot * K4_UNIT;
    const char* p = abase + st * 128;
#pragma unroll
    for (int i = 0; i < 4; ++i) {
      glds16(p + (size_t)i * 16 * F_ * 2, voffA0, sb + i * 2048);
      glds16(p + (size_t)i * 16 * F_ * 2, voffA1, sb + i * 2048 + 1024);
    }
  };
  auto issueB = [&](int st, int slot) {
    const unsigned sb = wbu + slot * K4_UNIT;
    const char* p = bbase + (size_t)st * 64 * D_ * 2;
#pragma unroll
    for (int i = 0; i < 8; ++i) glds16(p + i * 8 * D_ * 2, voffB, sb + i * 1024);
  };
  issueA(0, 0); issueB(0, 1); issueA(1, 2); issueB(1, 3); issueA(2, 4);
  const u32x4 hr0 = *(const u32x4*)(hT + (n0 + lane) * S_ + m0 + 16 * w);
  const u32x4 hr1 = *(const u32x4*)(hT + (n0 + lane) * S_ + m0 + 16 * w + 8);
  f32x16 acc[2][2];
#pragma unroll
  for (int i = 0; i < 2; ++i)
#pragma unroll
    for (int j = 0; j < 2; ++j)
#pragma unroll
      for (int e = 0; e < 16; ++e) acc[i][j][e] = 0.f;
  const int hf = lane >> 5, r32 = lane & 31;
  const int asw = (r32 >> 1) & 7;
  const int bq = (lane & 15) >> 2;
  const int bsw = ((bq >> 1) & 1) << 2;
  const int bcol = 16 * ((lane >> 4) & 1) + 4 * (lane & 3);
  const int boff0 = (8 * hf + bq) * 128 + (((bcol >> 3) ^ bsw) * 16) + (bcol & 7) * 2;
  const int boff1 = (8 * hf + bq) * 128 + ((((bcol + 32) >> 3) ^ bsw) * 16) + (bcol & 7) * 2;

  int sa = 0;
#pragma unroll 1
  for (int st = 0; st < 7; ++st) {
    if (st == 0) STAMP(3,1);
    if (st < 5) WAITV(24); else if (st == 5) WAITV(16); else WAITV(0);
    if (st == 0) STAMP(3,2);
    if (st == 1) STAMP(3,3);
    if (st == 4) STAMP(3,4);
    const int sbt = sa == 4 ? 0 : sa + 1;
    const lds_u8* pa = wb + sa * K4_UNIT;
    const lds_u8* pb = wb + sbt * K4_UNIT;
#pragma unroll
    for (int ks = 0; ks < 4; ++ks) {
      bf16x8 af[2], bf[2];
#pragma unroll
      for (int mb = 0; mb < 2; ++mb)
        af[mb] = *(const __attribute__((address_space(3))) bf16x8*)(pa + (r32 + 32 * mb) * 128 + (((2 * ks + hf) ^ asw) * 16));
      bf[0] = tr_frag(pb + boff0 + ks * 16 * 128, 4 * 128);
      bf[1] = tr_frag(pb + boff1 + ks * 16 * 128, 4 * 128);
#pragma unroll
      for (int mb = 0; mb < 2; ++mb)
#pragma unroll
        for (int nb = 0; nb < 2; ++nb)
          acc[mb][nb] = __builtin_amdgcn_mfma_f32_32x32x16_bf16(af[mb], bf[nb], acc[mb][nb], 0, 0, 0);
    }
    if (st + 2 < 7) issueB(st + 2, sa);
    if (st + 3 < 7) issueA(st + 3, sbt);
    sa = sbt == 4 ? 0 : sbt + 1;
  }
  STAMP(3,5);
  __syncthreads();
  float* red = (float*)smem_;
#pragma unroll
  for (int mb = 0; mb < 2; ++mb)
#pragma unroll
    for (int nb = 0; nb < 2; ++nb)
#pragma unroll
      for (int g = 0; g < 4; ++g)
        *(f32x4*)(red + w * 4352 + (32 * nb + r32) * 68 + 32 * mb + 8 * g + 4 * hf) =
            (f32x4){acc[mb][nb][4 * g], acc[mb][nb][4 * g + 1], acc[mb][nb][4 * g + 2], acc[mb][nb][4 * g + 3]};
  __syncthreads();
  u16* ob = (u16*)(smem_ + 71680);
#pragma unroll
  for (int i = 0; i < 4; ++i) {
    const float* rp = red + lane * 68 + 16 * w + 4 * i;
    const f32x4 s = (*(const f32x4*)(rp) + *(const f32x4*)(rp + 4352)) + (*(const f32x4*)(rp + 8704) + *(const f32x4*)(rp + 13056));
    const unsigned h0 = i < 2 ? hr0[2 * i] : hr1[2 * i - 4];
    const unsigned h1 = i < 2 ? hr0[2 * i + 1] : hr1[2 * i - 3];
    const unsigned o0 = pk2(unpk(h0) + rbf2((f32x2){s[0], s[1]}));
    const unsigned o1 = pk2(unpk(h1) + rbf2((f32x2){s[2], s[3]}));
    u16* op = ob + (16 * w + 4 * i) * 72 + lane;
    op[0] = (u16)o0; op[72] = (u16)(o0 >> 16); op[144] = (u16)o1; op[216] = (u16)(o1 >> 16);
  }
  __syncthreads();
#pragma unroll
  for (int i = 0; i < 2; ++i) {
    const int p = t + 256 * i, row = p >> 3, c = p & 7;
    *(u32x4*)(out + (m0 + row) * D_ + n0 + c * 8) = *(const u32x4*)(ob + row * 72 + c * 8);
  }
  STAMP(3,6);
  DRAIN();
  STAMP(3,7);
}

extern "C" void kernel_launch(void* const* d_in, const int* in_sizes, int n_in, void* d_out, int out_size,
                              void* d_ws, size_t ws_size, hipStream_t stream) {
  const u16* q = (const u16*)d_in[0];
  const u16* k = (const u16*)d_in[1];
  const u16* v = (const u16*)d_in[2];
  const u16* shk = (const u16*)d_in[3];
  const u16* ssmk = (const u16*)d_in[4];
  const u16* ssmD = (const u16*)d_in[5];
  const u16* wg = (const u16*)d_in[7];
  const u16* wu = (const u16*)d_in[8];
  const u16* wd = (const u16*)d_in[9];
  unsigned char* ws = (unsigned char*)d_ws;
  u16* kvR = (u16*)(ws);
  u16* xT = (u16*)(ws + (2u << 20));
  float* qm = (float*)(ws + (4u << 20));
  float* qinv = qm + 2048;
  float* csp = (float*)(ws + (4u << 20) + 65536);
  u16* h = (u16*)(ws + (5u << 20));
  u16* hh = (u16*)(ws + (8u << 20));
  u16* wp = (u16*)(ws + (16u << 20));
  k1_prep<<<512, 512, 0, stream>>>(q, k, v, shk, wg, wu, kvR, xT, qm, qinv, wp, csp);
  k2_conv<<<256, 512, 0, stream>>>(ssmk, ssmD, kvR, xT, qm, qinv, h);
  k3_gemm1<<<256, 512, 0, stream>>>(h, wp, csp, hh);
  k4_gemm2<<<256, 256, 0, stream>>>(hh, wd, h, (u16*)d_out);
}
```

```cpp
#include <hip/hip_runtime.h>
#include <hip/hip_bf16.h>
#include <stdint.h>

typedef unsigned short u16;
typedef __attribute__((ext_vector_type(8))) short bf16x8;
typedef __attribute__((ext_vector_type(4))) short s16x4;
typedef __attribute__((ext_vector_type(16))) float f32x16;
typedef __attribute__((ext_vector_type(4))) float f32x4;
typedef __attribute__((ext_vector_type(2))) float f32x2;
typedef __attribute__((ext_vector_type(4))) unsigned u32x4;
typedef __attribute__((ext_vector_type(2))) unsigned u32x2;
typedef __attribute__((ext_vector_type(2))) __bf16 bf16x2_t;
typedef __attribute__((ext_vector_type(4))) short v4i16_t;
typedef __attribute__((address_space(3))) unsigned char lds_u8;

#define DEV __device__ __forceinline__
#define STAMP(kid,i)
#define DRAIN()

static constexpr int S_ = 2048, D_ = 512, F_ = 1792;

DEV float bflo(unsigned x) { return __uint_as_float(x << 16); }
DEV float bfhi(unsigned x) { return __uint_as_float(x & 0xffff0000u); }
DEV f32x2 unpk(unsigned x) { return (f32x2){bflo(x), bfhi(x)}; }
DEV unsigned pk2(f32x2 v) {
  bf16x2_t r = {(__bf16)v[0], (__bf16)v[1]};
  return __builtin_bit_cast(unsigned, r);
}
DEV f32x2 rbf2(f32x2 v) { return unpk(pk2(v)); }
DEV float rbf(float x) { return (float)(__bf16)x; }

DEV void glds16(const void* sbase, unsigned voff, unsigned lds_dst) {
  asm volatile("s_mov_b32 m0, %2\n\ts_nop 0\n\tglobal_load_lds_dwordx4 %0, %1"
               :: "v"(voff), "s"(sbase), "s"(lds_dst) : "memory");
}
#define WAITV(n) asm volatile("s_waitcnt vmcnt(" #n ")" ::: "memory")

DEV s16x4 tr_read(const lds_u8* p) {
  return __builtin_bit_cast(s16x4, __builtin_amdgcn_ds_read_tr16_b64_v4i16((__attribute__((address_space(3))) v4i16_t*)p));
}
DEV bf16x8 tr_frag(const lds_u8* p, int rowstride4) {
  const s16x4 lo = tr_read(p);
  const s16x4 hi = tr_read(p + rowstride4);
  return (bf16x8){lo[0], lo[1], lo[2], lo[3], hi[0], hi[1], hi[2], hi[3]};
}

__global__ __launch_bounds__(512) void k1_prep(const u16* __restrict__ q, const u16* __restrict__ k,
                                               const u16* __restrict__ v, const u16* __restrict__ shk,
                                               const u16* __restrict__ wg, const u16* __restrict__ wu,
                                               u16* __restrict__ kvR, u16* __restrict__ xT,
                                               float* __restrict__ qm, float* __restrict__ qinv,
                                               u16* __restrict__ wp, float* __restrict__ csp,
                                               const u16* __restrict__ ssmk, const u16* __restrict__ wd,
                                               unsigned* __restrict__ sink) {
  __shared__ __attribute__((aligned(16))) unsigned char smem[28672 + 14336];
  const int b = blockIdx.x, t = threadIdx.x;
  if (b >= 512) {
    const int pb = b - 512;
    const u32x4* src = pb < 32 ? (const u32x4*)ssmk + pb * 4096 : (const u32x4*)wd + (pb - 32) * 4096;
    unsigned a = 0;
#pragma unroll
    for (int i = 0; i < 8; ++i) { const u32x4 x = src[i * 512 + t]; a |= x[0] ^ x[1] ^ x[2] ^ x[3]; }
    if (a == 0x9e3779b9u) sink[t] = a;
  } else if (b < 256) {
    u16* kvs = (u16*)smem;
    u16* xs = (u16*)(smem + 64 * 66 * 2);
    const int s0 = (b & 31) * 64, d0 = (b >> 5) * 64;
    const int c = t & 7, r = t >> 3, s = s0 + r;
    u32x4 wv[4], kr[4];
#pragma unroll
    for (int i = 0; i < 4; ++i) wv[i] = *(const u32x4*)(shk + (d0 + 8 * c) * 4 + 8 * i);
#pragma unroll
    for (int j = 0; j < 4; ++j) {
      const int ss = s - 3 + j;
      u32x4 z = {0u, 0u, 0u, 0u};
      kr[j] = (ss >= 0) ? *(const u32x4*)(k + ss * D_ + d0 + 8 * c) : z;
    }
    const u32x4 vv = *(const u32x4*)(v + s * D_ + d0 + 8 * c);
    const u32x4 qq = *(const u32x4*)(q + s * D_ + d0 + 8 * c);
#pragma unroll
    for (int p = 0; p < 4; ++p) {
      const f32x2 w0 = {bflo(wv[p][0]), bflo(wv[p][2])}, w1 = {bfhi(wv[p][0]), bfhi(wv[p][2])};
      const f32x2 w2 = {bflo(wv[p][1]), bflo(wv[p][3])}, w3 = {bfhi(wv[p][1]), bfhi(wv[p][3])};
      f32x2 ks = rbf2(unpk(kr[0][p]) * w3);
      ks = rbf2(ks + rbf2(unpk(kr[1][p]) * w2));
      ks = rbf2(ks + rbf2(unpk(kr[2][p]) * w1));
      ks = rbf2(ks + rbf2(unpk(kr[3][p]) * w0));
      const unsigned kvb = pk2(ks * unpk(vv[p]));
      kvs[(8 * c + 2 * p) * 66 + (63 - r)] = (u16)kvb;
      kvs[(8 * c + 2 * p + 1) * 66 + (63 - r)] = (u16)(kvb >> 16);
      xs[(8 * c + 2 * p) * 66 + r] = (u16)qq[p];
      xs[(8 * c + 2 * p + 1) * 66 + r] = (u16)(qq[p] >> 16);
    }
    __syncthreads();
    {
      const int ch = r;
      const unsigned* a = (const unsigned*)(kvs + ch * 66 + 8 * c);
      const unsigned* bq = (const unsigned*)(xs + ch * 66 + 8 * c);
      u32x4 o1 = {a[0], a[1], a[2], a[3]};
      u32x4 o2 = {bq[0], bq[1], bq[2], bq[3]};
      *(u32x4*)(kvR + (d0 + ch) * S_ + (1984 - s0) + 8 * c) = o1;
      *(u32x4*)(xT + (d0 + ch) * S_ + s0 + 8 * c) = o2;
    }
  } else if (b < 384) {
    const int lane = t & 63, w = t >> 6;
#pragma unroll
    for (int i = 0; i < 2; ++i) {
      const int row = (b - 256) * 16 + w * 2 + i;
      const u32x4 x = *(const u32x4*)(q + row * D_ + lane * 8);
      f32x2 f[4];
#pragma unroll
      for (int j = 0; j < 4; ++j) f[j] = unpk(x[j]);
      f32x2 s2 = (f[0] + f[1]) + (f[2] + f[3]);
      float sum = s2[0] + s2[1];
#pragma unroll
      for (int o = 32; o >= 1; o >>= 1) sum += __shfl_xor(sum, o);
      const float mean = sum * (1.0f / D_);
      f32x2 q2 = {0.f, 0.f};
#pragma unroll
      for (int j = 0; j < 4; ++j) { const f32x2 d = f[j] - mean; q2 += d * d; }
      float sq = q2[0] + q2[1];
#pragma unroll
      for (int o = 32; o >= 1; o >>= 1) sq += __shfl_xor(sq, o);
      const float var = rbf(sq * (1.0f / D_));
      if (lane == 0) {
        qm[row] = rbf(mean);
        qinv[row] = 1.0f / rbf(sqrtf(rbf(var + 1e-5f)));
      }
    }
  } else {
    lds_u8* tile = (lds_u8*)smem;
    float* red = (float*)(smem + 28672);
    const int bb = b - 384, ft = bb & 15, kc = bb >> 4;
    const int part = t % 28, rg = t / 28;
    if (rg < 16) {
      f32x2 acc[4] = {{0.f, 0.f}, {0.f, 0.f}, {0.f, 0.f}, {0.f, 0.f}};
      const int mat = part >= 14;
      const u16* src = (mat ? wu : wg) + ft * 112 + (part - 14 * mat) * 8;
#pragma unroll
      for (int i = 0; i < 4; ++i) {
        const int kk = rg * 4 + i;
        const u32x4 x = *(const u32x4*)(src + (kc * 64 + kk) * F_);
        *(u32x4*)(smem + kk * 448 + part * 16) = x;
#pragma unroll
        for (int j = 0; j < 4; ++j) acc[j] += unpk(x[j]);
      }
      *(f32x4*)(red + rg * 224 + part * 8) = (f32x4){acc[0][0], acc[0][1], acc[1][0], acc[1][1]};
      *(f32x4*)(red + rg * 224 + part * 8 + 4) = (f32x4){acc[2][0], acc[2][1], acc[3][0], acc[3][1]};
    }
    __syncthreads();
    const int lane = t & 63, w = t >> 6;
    const int woff = (8 * (lane >> 5) + ((lane & 15) >> 2)) * 448 + ((lane >> 4) & 1) * 224 + (lane & 3) * 8;
#pragma unroll 1
    for (int f = w; f < 28; f += 8) {
      const int j = f >> 2, ks = f & 3;
      const bf16x8 fr = tr_frag(tile + woff + ks * 16 * 448 + j * 32, 4 * 448);
      *(bf16x8*)(wp + ((((size_t)ft * 7 + j) * 32 + kc * 4 + ks) * 64 + lane) * 8) = fr;
    }
    if (t < 224) {
      float s = 0.f;
#pragma unroll
      for (int i = 0; i < 16; ++i) s += red[i * 224 + t];
      csp[(kc * 16 + ft) * 224 + t] = s;
    }
  }
}

static constexpr int K2_COPY = 4160;
static constexpr int K2_KC = 8 * K2_COPY;
static constexpr int K2_XR = 96 * 80;
static constexpr int K2_CH = K2_KC + K2_XR;

__global__ __launch_bounds__(512) void k2_conv(const u16* __restrict__ ssmk, const u16* __restrict__ ssmD,
                                               const u16* __restrict__ kvR, const u16* __restrict__ xT,
                                               const float* __restrict__ qm, const float* __restrict__ qinv,
                                               u16* __restrict__ hT) {
  __shared__ __attribute__((aligned(16))) unsigned char smem[2 * K2_CH];
  const int t = threadIdx.x, lane = t & 63, w = t >> 6;
  const int d0 = blockIdx.x * 2;
  const u32x4 z4 = {0u, 0u, 0u, 0u};
  const int ch = w >> 2, qq = w & 3;
  const int il = lane & 7, ih = (lane >> 3) & 3, hh = lane >> 5, col = lane & 31;
  const int d = d0 + ch;
  STAMP(1,0);
  const int tv = t & 255;
  const u16* kk = ssmk + d * S_;
  u32x4 cur = *(const u32x4*)(kk + tv * 8);
  u32x4 nxt = (tv < 255) ? *(const u32x4*)(kk + tv * 8 + 8) : z4;
  const u32x4 xv = *(const u32x4*)(kvR + d * S_ + tv * 8);
  const int nb0 = 1024 * (qq >> 1) + 32 * col + 16 * (qq & 1) + 4 * hh;
  u32x2 xq[2];
  f32x4 mm[2], dd[2];
#pragma unroll
  for (int e = 0; e < 2; ++e) {
    xq[e] = *(const u32x2*)(xT + d * S_ + nb0 + 8 * e);
    mm[e] = *(const f32x4*)(qm + nb0 + 8 * e);
    dd[e] = *(const f32x4*)(qinv + nb0 + 8 * e);
  }
  const float Dd = bflo(ssmD[d]);
  unsigned char* base = smem + ch * K2_CH;
  *(u32x4*)(base + K2_KC + (tv >> 2) * 80 + (tv & 3) * 16) = xv;
  if (tv < 128) *(u32x4*)(base + K2_KC + (64 + (tv >> 2)) * 80 + (tv & 3) * 16) = z4;
  int unit = tv + 4;
#pragma unroll 1
  for (int rep = 0; rep < 2; ++rep) {
    const unsigned wd[8] = {cur[0], cur[1], cur[2], cur[3], nxt[0], nxt[1], nxt[2], nxt[3]};
#pragma unroll
    for (int sh = 1; sh <= 8; ++sh) {
      u32x4 o;
      const int a = sh >> 1;
#pragma unroll
      for (int i = 0; i < 4; ++i) o[i] = (sh & 1) ? __builtin_amdgcn_alignbit(wd[a + i + 1], wd[a + i], 16) : wd[a + i];
      *(u32x4*)(base + (sh - 1) * K2_COPY + unit * 16) = o;
    }
    if (tv >= 4) break;
    nxt = (tv == 0) ? cur : z4;
    cur = z4;
    unit = (tv + 3) & 3;
  }
  STAMP(1,1);
  __syncthreads();
  STAMP(1,2);
  const unsigned char* kcb = base + il * K2_COPY + (2 + ih + hh + 32 * qq) * 16;
  const unsigned char* xb = base + K2_KC + (63 - col + 8 * qq) * 80 + (16 + 8 * hh) * 2;
  f32x16 acc0, acc1;
#pragma unroll
  for (int i = 0; i < 16; ++i) { acc0[i] = 0.f; acc1[i] = 0.f; }
#pragma unroll 2
  for (int c = 0; c < 8; ++c) {
#pragma unroll
    for (int s = 0; s < 2; ++s) {
      const bf16x8 a0 = *(const bf16x8*)(kcb + (4 * c - 2 * s) * 16);
      const bf16x8 a1 = *(const bf16x8*)(kcb + (4 * c - 2 * s + 128) * 16);
      const bf16x8 b0 = *(const bf16x8*)(xb + c * 80 - 32 * s);
      const bf16x8 b1 = *(const bf16x8*)(xb + (c - 32) * 80 - 32 * s);
      acc0 = __builtin_amdgcn_mfma_f32_32x32x16_bf16(a0, b0, acc0, 0, 0, 0);
      acc1 = __builtin_amdgcn_mfma_f32_32x32x16_bf16(a0, b1, acc1, 0, 0, 0);
      acc1 = __builtin_amdgcn_mfma_f32_32x32x16_bf16(a1, b0, acc1, 0, 0, 0);
    }
  }
  STAMP(1,3);
  __syncthreads();
  f32x4* red = (f32x4*)base;
#pragma unroll
  for (int g = 0; g < 4; ++g) {
    red[(qq * 8 + g) * 64 + lane] = (f32x4){acc0[4 * g], acc0[4 * g + 1], acc0[4 * g + 2], acc0[4 * g + 3]};
    red[(qq * 8 + 4 + g) * 64 + lane] = (f32x4){acc1[4 * g], acc1[4 * g + 1], acc1[4 * g + 2], acc1[4 * g + 3]};
  }
  __syncthreads();
  STAMP(1,4);
  unsigned pk[2][2];
#pragma unroll
  for (int e = 0; e < 2; ++e) {
    const f32x4* rp = red + (2 * qq + e) * 64 + lane;
    const f32x4 y4 = (rp[0] + rp[512]) + (rp[1024] + rp[1536]);
    const int t0 = 2044 - (nb0 + 8 * e);
    const u32x2 kvp = *(const u32x2*)(base + K2_KC + (t0 >> 5) * 80 + (t0 & 31) * 2);
#pragma unroll
    for (int r = 0; r < 2; ++r) {
      const f32x2 kvf = {bfhi(kvp[1 - r]), bflo(kvp[1 - r])};
      const f32x2 xf = unpk(xq[e][r]);
      const f32x2 yy = rbf2((f32x2){y4[2 * r], y4[2 * r + 1]});
      const f32x2 y2 = rbf2(yy + rbf2(kvf * Dd));
      const f32x2 qn = rbf2(rbf2(xf - (f32x2){mm[e][2 * r], mm[e][2 * r + 1]}) * (f32x2){dd[e][2 * r], dd[e][2 * r + 1]});
      pk[e][r] = pk2(xf + rbf2(qn * y2));
    }
  }
  auto s0 = __builtin_amdgcn_permlane32_swap(pk[0][0], pk[1][0], false, false);
  auto s1 = __builtin_amdgcn_permlane32_swap(pk[0][1], pk[1][1], false, false);
  const u32x4 ov = {s0[0], s1[0], s0[1], s1[1]};
  STAMP(1,5);
  *(u32x4*)(hT + d * S_ + 1024 * (qq >> 1) + 32 * col + 16 * (qq & 1) + 8 * hh) = ov;
  STAMP(1,6);
  DRAIN();
  STAMP(1,7);
}

DEV void gload16(bf16x8& dst, const void* sbase, unsigned voff) {
  asm volatile("global_load_dwordx4 %0, %1, %2" : "=v"(dst) : "v"(voff), "s"(sbase) : "memory");
}

template <bool STATS>
DEV void k3_main(lds_u8* smem, const u16* wp, int ft, int w, int lane, float* stl, f32x16* acc) {
  const int hf = lane >> 5, q4 = (lane & 15) >> 2;
  {
    const int bsl = 2 * ((lane >> 4) & 1) + ((lane & 3) >> 1);
    int boff[4];
#pragma unroll
    for (int i = 0; i < 4; ++i)
      boff[i] = (8 * hf + q4) * 256 + (((4 * ((i + w) & 3) + bsl) ^ (q4 << 2)) * 16) + (lane & 1) * 8;
    const char* wbase = (const char*)(wp + ((size_t)ft * 7 + w) * 32 * 512);
    const unsigned voffW = lane * 16;
    bf16x8 wf[32];
#pragma unroll
    for (int i = 0; i < 12; ++i) gload16(wf[i], wbase + i * 1024, voffW);
#pragma unroll
    for (int j = 0; j < 4; ++j)
#pragma unroll
      for (int i = 0; i < 16; ++i) acc[j][i] = 0.f;
    float sum0 = 0.f, sum1 = 0.f, sq0 = 0.f, sq1 = 0.f;
    bf16x8 b0[4], b1[4];
    auto ld = [&](int kg, bf16x8* bb) {
#pragma unroll
      for (int i = 0; i < 4; ++i) bb[i] = tr_frag(smem + kg * 4096 + boff[i], 1024);
    };
    auto mm = [&](int kg, const bf16x8* bb) {
#pragma unroll
      for (int i = 0; i < 4; ++i) acc[i] = __builtin_amdgcn_mfma_f32_32x32x16_bf16(wf[kg], bb[i], acc[i], 0, 0, 0);
      if (STATS) {
        const u32x4 bu = __builtin_bit_cast(u32x4, bb[0]);
#pragma unroll
        for (int i = 0; i < 4; ++i) {
          const unsigned xi = bu[i];
          const float lo = bflo(xi), hi = bfhi(xi);
          sum0 += lo; sum1 += hi;
          sq0 = fmaf(lo, lo, sq0); sq1 = fmaf(hi, hi, sq1);
        }
        asm volatile("" : "+v"(sum0), "+v"(sum1), "+v"(sq0), "+v"(sq1));
      }
    };
    STAMP(2,1);
    asm volatile("s_waitcnt vmcnt(8)" : "+v"(wf[0]), "+v"(wf[1]), "+v"(wf[2]), "+v"(wf[3]) :: "memory");
    __builtin_amdgcn_s_barrier();
    STAMP(2,2);
    ld(0, b0);
#pragma unroll
    for (int c = 0; c < 8; ++c) {
      if (c < 5) {
#pragma unroll
        for (int i = 0; i < 4; ++i) gload16(wf[4 * c + 12 + i], wbase + (4 * c + 12 + i) * 1024, voffW);
      }
      ld(4 * c + 1, b1); mm(4 * c, b0);
      ld(4 * c + 2, b0); mm(4 * c + 1, b1);
      ld(4 * c + 3, b1); mm(4 * c + 2, b0);
      if (c < 7) {
        asm volatile("s_waitcnt vmcnt(%4)" : "+v"(wf[4 * c + 4]), "+v"(wf[4 * c + 5]), "+v"(wf[4 * c + 6]), "+v"(wf[4 * c + 7])
                     : "i"(c < 5 ? 8 : (c == 5 ? 4 : 0)) : "memory");
        __builtin_amdgcn_s_barrier();
        if (c == 0) STAMP(2,3);
        if (c == 3) STAMP(2,4);
        ld(4 * c + 4, b0);
      }
      mm(4 * c + 3, b1);
    }
    STAMP(2,5);
    if (STATS) {
      float sum = sum0 + sum1, sq = sq0 + sq1;
      sum += __shfl_xor(sum, 32);
      sq += __shfl_xor(sq, 32);
      const float mean = sum * (1.0f / D_);
      const float var = rbf(fmaxf(sq * (1.0f / D_) - mean * mean, 0.f));
      const float inv_ = 1.0f / rbf(sqrtf(rbf(var + 1e-5f)));
      if (lane < 32) *(f32x2*)(stl + (w * 32 + lane) * 2) = (f32x2){inv_, -rbf(mean) * inv_};
    }
  }
}

__global__ __launch_bounds__(512) void k3_gemm1(const u16* __restrict__ hT, const u16* __restrict__ wp,
                                                const float* __restrict__ csp, u16* __restrict__ hh) {
  __shared__ __attribute__((aligned(16))) unsigned char smem_[131072 + 2048 + 7168];
  lds_u8* smem = (lds_u8*)smem_;
  const unsigned sbase = (unsigned)(size_t)smem;
  const int t = threadIdx.x, lane = t & 63;
  const int w = __builtin_amdgcn_readfirstlane(t >> 6);
  const int b = blockIdx.x, x = b & 7, idx = b >> 3;
  const int mt = (x & 1) * 8 + (idx & 7), ft = (x >> 1) * 4 + (idx >> 3);
  const int m0 = mt * 128, f0 = ft * 112;
  STAMP(2,0);
  float* csl = (float*)(smem_ + 131072);
  float* stl = csl + 224;
  const int hf = lane >> 5;
  f32x16 acc[4];
  if (w < 4) {
    k3_main<true>(smem, wp, ft, w, lane, stl, acc);
  } else if (w < 7) {
    k3_main<false>(smem, wp, ft, w, lane, stl, acc);
  } else {
    const unsigned voffA = ((lane >> 4) * S_ + (((lane & 15) ^ ((lane >> 4) << 2)) * 8)) * 2;
    const char* abase = (const char*)(hT + m0);
    auto issue = [&](int c) {
#pragma unroll
      for (int i = 0; i < 16; ++i) glds16(abase + (size_t)(c * 64 + 4 * i) * S_ * 2, voffA, sbase + c * 16384 + i * 1024);
    };
    issue(0); issue(1); issue(2);
#pragma unroll 1
    for (int c = 0; c < 8; ++c) {
      if (c < 6) WAITV(32); else if (c == 6) WAITV(16); else WAITV(0);
      __builtin_amdgcn_s_barrier();
      if (c < 5) issue(c + 3);
    }
#pragma unroll 1
    for (int i = lane; i < 224; i += 64) {
      float s = 0.f;
#pragma unroll
      for (int k8 = 0; k8 < 8; ++k8) s += csp[(k8 * 16 + ft) * 224 + i];
      csl[i] = s;
    }
  }
  __syncthreads();
  if (w < 7) {
    f32x4 cg[2], cu[2];
#pragma unroll
    for (int g = 0; g < 2; ++g) {
      cg[g] = *(const f32x4*)(csl + 16 * w + 8 * g + 4 * hf);
      cu[g] = *(const f32x4*)(csl + 112 + 16 * w + 8 * g + 4 * hf);
    }
    u16* orow = hh + (size_t)(m0 + (lane & 31)) * F_ + f0 + 16 * w + 8 * hf;
#pragma unroll
    for (int i = 0; i < 4; ++i) {
      const int mb = (i + w) & 3;
      const f32x2 st2 = *(const f32x2*)(stl + (mb * 32 + (lane & 31)) * 2);
      const float inv = st2[0], mi = st2[1];
      unsigned o[4];
#pragma unroll
      for (int g = 0; g < 2; ++g) {
#pragma unroll
        for (int r = 0; r < 2; ++r) {
          const f32x2 ag = {acc[i][4 * g + 2 * r], acc[i][4 * g + 2 * r + 1]};
          const f32x2 au = {acc[i][8 + 4 * g + 2 * r], acc[i][8 + 4 * g + 2 * r + 1]};
          f32x2 gt = ag * inv + (f32x2){cg[g][2 * r], cg[g][2 * r + 1]} * mi;
          const f32x2 up = au * inv + (f32x2){cu[g][2 * r], cu[g][2 * r + 1]} * mi;
          gt[0] = gt[0] > 0.f ? gt[0] : 0.f;
          gt[1] = gt[1] > 0.f ? gt[1] : 0.f;
          o[2 * g + r] = pk2(gt * up);
        }
      }
      auto s0 = __builtin_amdgcn_permlane32_swap(o[0], o[2], false, false);
      auto s1 = __builtin_amdgcn_permlane32_swap(o[1], o[3], false, false);
      u32x4 ov = {s0[0], s1[0], s0[1], s1[1]};
      *(u32x4*)(orow + (size_t)mb * 32 * F_) = ov;
    }
  }
  STAMP(2,6);
  DRAIN();
  STAMP(2,7);
}

static constexpr int K4_UNIT = 8192, K4_WAVE = 5 * K4_UNIT;

__global__ __launch_bounds__(256) void k4_gemm2(const u16* __restrict__ hh, const u16* __restrict__ wd,
                                                const u16* __restrict__ hT, u16* __restrict__ out) {
  __shared__ __attribute__((aligned(16))) unsigned char smem_[4 * K4_WAVE];
  lds_u8* smem = (lds_u8*)smem_;
  const int t = threadIdx.x, lane = t & 63;
  const int w = __builtin_amdgcn_readfirstlane(t >> 6);
  const int b = blockIdx.x, x = b & 7, idx = b >> 3;
  const int mt = x * 4 + (idx & 3), nt = idx >> 2;
  const int m0 = mt * 64, n0 = nt * 64;
  STAMP(3,0);
  lds_u8* wb = smem + w * K4_WAVE;
  const unsigned wbu = (unsigned)(size_t)wb;
  const int prow = lane >> 3, pslot = lane & 7;
  const unsigned voffA0 = (prow * F_ + (pslot ^ ((prow >> 1) & 7)) * 8) * 2;
  const unsigned voffA1 = ((prow + 8) * F_ + (pslot ^ (((prow >> 1) + 4) & 7)) * 8) * 2;
  const unsigned voffB = (prow * D_ + (pslot ^ (((prow >> 1) & 1) << 2)) * 8) * 2;
  const char* abase = (const char*)(hh + (size_t)m0 * F_ + w * 448);
  const char* bbase = (const char*)(wd + (size_t)w * 448 * D_ + n0);
  auto issueA = [&](int st, int slot) {
    const unsigned sb = wbu + slot * K4_UNIT;
    const char* p = abase + st * 128;
#pragma unroll
    for (int i = 0; i < 4; ++i) {
      glds16(p + (size_t)i * 16 * F_ * 2, voffA0, sb + i * 2048);
      glds16(p + (size_t)i * 16 * F_ * 2, voffA1, sb + i * 2048 + 1024);
    }
  };
  auto issueB = [&](int st, int slot) {
    const unsigned sb = wbu + slot * K4_UNIT;
    const char* p = bbase + (size_t)st * 64 * D_ * 2;
#pragma unroll
    for (int i = 0; i < 8; ++i) glds16(p + i * 8 * D_ * 2, voffB, sb + i * 1024);
  };
  issueA(0, 0); issueB(0, 1); issueA(1, 2); issueB(1, 3); issueA(2, 4);
  const u32x4 hr0 = *(const u32x4*)(hT + (n0 + lane) * S_ + m0 + 16 * w);
  const u32x4 hr1 = *(const u32x4*)(hT + (n0 + lane) * S_ + m0 + 16 * w + 8);
  f32x16 acc[2][2];
#pragma unroll
  for (int i = 0; i < 2; ++i)
#pragma unroll
    for (int j = 0; j < 2; ++j)
#pragma unroll
      for (int e = 0; e < 16; ++e) acc[i][j][e] = 0.f;
  const int hf = lane >> 5, r32 = lane & 31;
  const int asw = (r32 >> 1) & 7;
  const int bq = (lane & 15) >> 2;
  const int bsw = ((bq >> 1) & 1) << 2;
  const int bcol = 16 * ((lane >> 4) & 1) + 4 * (lane & 3);
  const int boff0 = (8 * hf + bq) * 128 + (((bcol >> 3) ^ bsw) * 16) + (bcol & 7) * 2;
  const int boff1 = (8 * hf + bq) * 128 + ((((bcol + 32) >> 3) ^ bsw) * 16) + (bcol & 7) * 2;

  int sa = 0;
#pragma unroll 1
  for (int st = 0; st < 7; ++st) {
    if (st == 0) STAMP(3,1);
    if (st < 5) WAITV(24); else if (st == 5) WAITV(16); else WAITV(0);
    if (st == 0) STAMP(3,2);
    if (st == 1) STAMP(3,3);
    if (st == 4) STAMP(3,4);
    const int sbt = sa == 4 ? 0 : sa + 1;
    const lds_u8* pa = wb + sa * K4_UNIT;
    const lds_u8* pb = wb + sbt * K4_UNIT;
#pragma unroll
    for (int ks = 0; ks < 4; ++ks) {
      bf16x8 af[2], bf[2];
#pragma unroll
      for (int mb = 0; mb < 2; ++mb)
        af[mb] = *(const __attribute__((address_space(3))) bf16x8*)(pa + (r32 + 32 * mb) * 128 + (((2 * ks + hf) ^ asw) * 16));
      bf[0] = tr_frag(pb + boff0 + ks * 16 * 128, 4 * 128);
      bf[1] = tr_frag(pb + boff1 + ks * 16 * 128, 4 * 128);
#pragma unroll
      for (int mb = 0; mb < 2; ++mb)
#pragma unroll
        for (int nb = 0; nb < 2; ++nb)
          acc[mb][nb] = __builtin_amdgcn_mfma_f32_32x32x16_bf16(af[mb], bf[nb], acc[mb][nb], 0, 0, 0);
    }
    if (st + 2 < 7) issueB(st + 2, sa);
    if (st + 3 < 7) issueA(st + 3, sbt);
    sa = sbt == 4 ? 0 : sbt + 1;
  }
  STAMP(3,5);
  __syncthreads();
  float* red = (float*)smem_;
#pragma unroll
  for (int mb = 0; mb < 2; ++mb)
#pragma unroll
    for (int nb = 0; nb < 2; ++nb)
#pragma unroll
      for (int g = 0; g < 4; ++g)
        *(f32x4*)(red + w * 4352 + (32 * nb + r32) * 68 + 32 * mb + 8 * g + 4 * hf) =
            (f32x4){acc[mb][nb][4 * g], acc[mb][nb][4 * g + 1], acc[mb][nb][4 * g + 2], acc[mb][nb][4 * g + 3]};
  __syncthreads();
  u16* ob = (u16*)(smem_ + 71680);
#pragma unroll
  for (int i = 0; i < 4; ++i) {
    const float* rp = red + lane * 68 + 16 * w + 4 * i;
    const f32x4 s = (*(const f32x4*)(rp) + *(const f32x4*)(rp + 4352)) + (*(const f32x4*)(rp + 8704) + *(const f32x4*)(rp + 13056));
    const unsigned h0 = i < 2 ? hr0[2 * i] : hr1[2 * i - 4];
    const unsigned h1 = i < 2 ? hr0[2 * i + 1] : hr1[2 * i - 3];
    const unsigned o0 = pk2(unpk(h0) + rbf2((f32x2){s[0], s[1]}));
    const unsigned o1 = pk2(unpk(h1) + rbf2((f32x2){s[2], s[3]}));
    u16* op = ob + (16 * w + 4 * i) * 72 + lane;
    op[0] = (u16)o0; op[72] = (u16)(o0 >> 16); op[144] = (u16)o1; op[216] = (u16)(o1 >> 16);
  }
  __syncthreads();
#pragma unroll
  for (int i = 0; i < 2; ++i) {
    const int p = t + 256 * i, row = p >> 3, c = p & 7;
    *(u32x4*)(out + (m0 + row) * D_ + n0 + c * 8) = *(const u32x4*)(ob + row * 72 + c * 8);
  }
  STAMP(3,6);
  DRAIN();
  STAMP(3,7);
}

extern "C" void kernel_launch(void* const* d_in, const int* in_sizes, int n_in, void* d_out, int out_size,
                              void* d_ws, size_t ws_size, hipStream_t stream) {
  const u16* q = (const u16*)d_in[0];
  const u16* k = (const u16*)d_in[1];
  const u16* v = (const u16*)d_in[2];
  const u16* shk = (const u16*)d_in[3];
  const u16* ssmk = (const u16*)d_in[4];
  const u16* ssmD = (const u16*)d_in[5];
  const u16* wg = (const u16*)d_in[7];
  const u16* wu = (const u16*)d_in[8];
  const u16* wd = (const u16*)d_in[9];
  unsigned char* ws = (unsigned char*)d_ws;
  u16* kvR = (u16*)(ws);
  u16* xT = (u16*)(ws + (2u << 20));
  float* qm = (float*)(ws + (4u << 20));
  float* qinv = qm + 2048;
  float* csp = (float*)(ws + (4u << 20) + 65536);
  u16* h = (u16*)(ws + (5u << 20));
  u16* hh = (u16*)(ws + (8u << 20));
  u16* wp = (u16*)(ws + (16u << 20));
  k1_prep<<<572, 512, 0, stream>>>(q, k, v, shk, wg, wu, kvR, xT, qm, qinv, wp, csp, ssmk, wd, (unsigned*)(ws + (24u << 20)));
  k2_conv<<<256, 512, 0, stream>>>(ssmk, ssmD, kvR, xT, qm, qinv, h);
  k3_gemm1<<<256, 512, 0, stream>>>(h, wp, csp, hh);
  k4_gemm2<<<256, 256, 0, stream>>>(hh, wd, h, (u16*)d_out);
}
```

```cpp
#include <hip/hip_runtime.h>
#include <hip/hip_bf16.h>
#include <stdint.h>

typedef unsigned short u16;
typedef __attribute__((ext_vector_type(8))) short bf16x8;
typedef __attribute__((ext_vector_type(4))) short s16x4;
typedef __attribute__((ext_vector_type(16))) float f32x16;
typedef __attribute__((ext_vector_type(4))) float f32x4;
typedef __attribute__((ext_vector_type(2))) float f32x2;
typedef __attribute__((ext_vector_type(4))) unsigned u32x4;
typedef __attribute__((ext_vector_type(2))) unsigned u32x2;
typedef __attribute__((ext_vector_type(2))) __bf16 bf16x2_t;
typedef __attribute__((ext_vector_type(4))) short v4i16_t;
typedef __attribute__((address_space(3))) unsigned char lds_u8;

#define DEV __device__ __forceinline__
#define STAMP(kid,i)
#define DRAIN()

static constexpr int S_ = 2048, D_ = 512, F_ = 1792;

DEV float bflo(unsigned x) { return __uint_as_float(x << 16); }
DEV float bfhi(unsigned x) { return __uint_as_float(x & 0xffff0000u); }
DEV f32x2 unpk(unsigned x) { return (f32x2){bflo(x), bfhi(x)}; }
DEV unsigned pk2(f32x2 v) {
  bf16x2_t r = {(__bf16)v[0], (__bf16)v[1]};
  return __builtin_bit_cast(unsigned, r);
}
DEV f32x2 rbf2(f32x2 v) { return unpk(pk2(v)); }
DEV float rbf(float x) { return (float)(__bf16)x; }

DEV void glds16(const void* sbase, unsigned voff, unsigned lds_dst) {
  asm volatile("s_mov_b32 m0, %2\n\ts_nop 0\n\tglobal_load_lds_dwordx4 %0, %1"
               :: "v"(voff), "s"(sbase), "s"(lds_dst) : "memory");
}
#define WAITV(n) asm volatile("s_waitcnt vmcnt(" #n ")" ::: "memory")

DEV s16x4 tr_read(const lds_u8* p) {
  return __builtin_bit_cast(s16x4, __builtin_amdgcn_ds_read_tr16_b64_v4i16((__attribute__((address_space(3))) v4i16_t*)p));
}
DEV bf16x8 tr_frag(const lds_u8* p, int rowstride4) {
  const s16x4 lo = tr_read(p);
  const s16x4 hi = tr_read(p + rowstride4);
  return (bf16x8){lo[0], lo[1], lo[2], lo[3], hi[0], hi[1], hi[2], hi[3]};
}

__global__ __launch_bounds__(512) void k1_prep(const u16* __restrict__ q, const u16* __restrict__ k,
                                               const u16* __restrict__ v, const u16* __restrict__ shk,
                                               u16* __restrict__ kvR, u16* __restrict__ xT,
                                               float* __restrict__ qm, float* __restrict__ qinv) {
  __shared__ __attribute__((aligned(16))) unsigned char smem[2 * 64 * 66 * 2];
  const int b = blockIdx.x, t = threadIdx.x;
  if (b < 256) {
    u16* kvs = (u16*)smem;
    u16* xs = (u16*)(smem + 64 * 66 * 2);
    const int s0 = (b & 31) * 64, d0 = (b >> 5) * 64;
    const int c = t & 7, r = t >> 3, s = s0 + r;
    u32x4 wv[4], kr[4];
#pragma unroll
    for (int i = 0; i < 4; ++i) wv[i] = *(const u32x4*)(shk + (d0 + 8 * c) * 4 + 8 * i);
#pragma unroll
    for (int j = 0; j < 4; ++j) {
      const int ss = s - 3 + j;
      u32x4 z = {0u, 0u, 0u, 0u};
      kr[j] = (ss >= 0) ? *(const u32x4*)(k + ss * D_ + d0 + 8 * c) : z;
    }
    const u32x4 vv = *(const u32x4*)(v + s * D_ + d0 + 8 * c);
    const u32x4 qq = *(const u32x4*)(q + s * D_ + d0 + 8 * c);
#pragma unroll
    for (int p = 0; p < 4; ++p) {
      const f32x2 w0 = {bflo(wv[p][0]), bflo(wv[p][2])}, w1 = {bfhi(wv[p][0]), bfhi(wv[p][2])};
      const f32x2 w2 = {bflo(wv[p][1]), bflo(wv[p][3])}, w3 = {bfhi(wv[p][1]), bfhi(wv[p][3])};
      f32x2 ks = rbf2(unpk(kr[0][p]) * w3);
      ks = rbf2(ks + rbf2(unpk(kr[1][p]) * w2));
      ks = rbf2(ks + rbf2(unpk(kr[2][p]) * w1));
      ks = rbf2(ks + rbf2(unpk(kr[3][p]) * w0));
      const unsigned kvb = pk2(ks * unpk(vv[p]));
      kvs[(8 * c + 2 * p) * 66 + (63 - r)] = (u16)kvb;
      kvs[(8 * c + 2 * p + 1) * 66 + (63 - r)] = (u16)(kvb >> 16);
      xs[(8 * c + 2 * p) * 66 + r] = (u16)qq[p];
      xs[(8 * c + 2 * p + 1) * 66 + r] = (u16)(qq[p] >> 16);
    }
    __syncthreads();
    {
      const int ch = r;
      const unsigned* a = (const unsigned*)(kvs + ch * 66 + 8 * c);
      const unsigned* bq = (const unsigned*)(xs + ch * 66 + 8 * c);
      u32x4 o1 = {a[0], a[1], a[2], a[3]};
      u32x4 o2 = {bq[0], bq[1], bq[2], bq[3]};
      *(u32x4*)(kvR + (d0 + ch) * S_ + (1984 - s0) + 8 * c) = o1;
      *(u32x4*)(xT + (d0 + ch) * S_ + s0 + 8 * c) = o2;
    }
  } else {
    const int lane = t & 63, w = t >> 6;
#pragma unroll
    for (int i = 0; i < 2; ++i) {
      const int row = (b - 256) * 16 + w * 2 + i;
      const u32x4 x = *(const u32x4*)(q + row * D_ + lane * 8);
      f32x2 f[4];
#pragma unroll
      for (int j = 0; j < 4; ++j) f[j] = unpk(x[j]);
      f32x2 s2 = (f[0] + f[1]) + (f[2] + f[3]);
      float sum = s2[0] + s2[1];
#pragma unroll
      for (int o = 32; o >= 1; o >>= 1) sum += __shfl_xor(sum, o);
      const float mean = sum * (1.0f / D_);
      f32x2 q2 = {0.f, 0.f};
#pragma unroll
      for (int j = 0; j < 4; ++j) { const f32x2 d = f[j] - mean; q2 += d * d; }
      float sq = q2[0] + q2[1];
#pragma unroll
      for (int o = 32; o >= 1; o >>= 1) sq += __shfl_xor(sq, o);
      const float var = rbf(sq * (1.0f / D_));
      if (lane == 0) {
        qm[row] = rbf(mean);
        qinv[row] = 1.0f / rbf(sqrtf(rbf(var + 1e-5f)));
      }
    }
  }
}

static constexpr int K2_COPY = 4160;
static constexpr int K2_KC = 8 * K2_COPY;
static constexpr int K2_XR = 96 * 80;
static constexpr int K2_CH = K2_KC + K2_XR;

__global__ __launch_bounds__(512) void k2_conv(const u16* __restrict__ ssmk, const u16* __restrict__ ssmD,
                                               const u16* __restrict__ kvR, const u16* __restrict__ xT,
                                               const float* __restrict__ qm, const float* __restrict__ qinv,
                                               u16* __restrict__ hT,
                                               const u16* __restrict__ wg, const u16* __restrict__ wu,
                                               u16* __restrict__ wp, float* __restrict__ csp,
                                               const u16* __restrict__ wd, unsigned* __restrict__ sink) {
  __shared__ __attribute__((aligned(16))) unsigned char smem[2 * K2_CH];
  const int t = threadIdx.x, lane = t & 63, w = t >> 6;
  if (blockIdx.x >= 384) {
    const u32x4* src = (const u32x4*)wd + (blockIdx.x - 384) * 4096;
    unsigned a = 0;
#pragma unroll
    for (int i = 0; i < 8; ++i) { const u32x4 x = src[i * 512 + t]; a |= x[0] ^ x[1] ^ x[2] ^ x[3]; }
    if (a == 0x9e3779b9u) sink[t] = a;
    return;
  }
  if (blockIdx.x >= 256) {
    lds_u8* tile = (lds_u8*)smem;
    float* red = (float*)(smem + 28672);
    const int bb = blockIdx.x - 256, ft = bb & 15, kc = bb >> 4;
    const int part = t % 28, rg = t / 28;
    if (rg < 16) {
      f32x2 acc[4] = {{0.f, 0.f}, {0.f, 0.f}, {0.f, 0.f}, {0.f, 0.f}};
      const int mat = part >= 14;
      const u16* src = (mat ? wu : wg) + ft * 112 + (part - 14 * mat) * 8;
#pragma unroll
      for (int i = 0; i < 4; ++i) {
        const int kk = rg * 4 + i;
        const u32x4 x = *(const u32x4*)(src + (kc * 64 + kk) * F_);
        *(u32x4*)(smem + kk * 448 + part * 16) = x;
#pragma unroll
        for (int j = 0; j < 4; ++j) acc[j] += unpk(x[j]);
      }
      *(f32x4*)(red + rg * 224 + part * 8) = (f32x4){acc[0][0], acc[0][1], acc[1][0], acc[1][1]};
      *(f32x4*)(red + rg * 224 + part * 8 + 4) = (f32x4){acc[2][0], acc[2][1], acc[3][0], acc[3][1]};
    }
    __syncthreads();
    const int woff = (8 * (lane >> 5) + ((lane & 15) >> 2)) * 448 + ((lane >> 4) & 1) * 224 + (lane & 3) * 8;
#pragma unroll 1
    for (int f = w; f < 28; f += 8) {
      const int j = f >> 2, ks = f & 3;
      const bf16x8 fr = tr_frag(tile + woff + ks * 16 * 448 + j * 32, 4 * 448);
      *(bf16x8*)(wp + ((((size_t)ft * 7 + j) * 32 + kc * 4 + ks) * 64 + lane) * 8) = fr;
    }
    if (t < 224) {
      float s = 0.f;
#pragma unroll
      for (int i = 0; i < 16; ++i) s += red[i * 224 + t];
      csp[(kc * 16 + ft) * 224 + t] = s;
    }
    return;
  }
  const int d0 = blockIdx.x * 2;
  const u32x4 z4 = {0u, 0u, 0u, 0u};
  const int ch = w >> 2, qq = w & 3;
  const int il = lane & 7, ih = (lane >> 3) & 3, hh = lane >> 5, col = lane & 31;
  const int d = d0 + ch;
  STAMP(1,0);
  const int tv = t & 255;
  const u16* kk = ssmk + d * S_;
  u32x4 cur = *(const u32x4*)(kk + tv * 8);
  u32x4 nxt = (tv < 255) ? *(const u32x4*)(kk + tv * 8 + 8) : z4;
  const u32x4 xv = *(const u32x4*)(kvR + d * S_ + tv * 8);
  const int nb0 = 1024 * (qq >> 1) + 32 * col + 16 * (qq & 1) + 4 * hh;
  u32x2 xq[2];
  f32x4 mm[2], dd[2];
#pragma unroll
  for (int e = 0; e < 2; ++e) {
    xq[e] = *(const u32x2*)(xT + d * S_ + nb0 + 8 * e);
    mm[e] = *(const f32x4*)(qm + nb0 + 8 * e);
    dd[e] = *(const f32x4*)(qinv + nb0 + 8 * e);
  }
  const float Dd = bflo(ssmD[d]);
  unsigned char* base = smem + ch * K2_CH;
  *(u32x4*)(base + K2_KC + (tv >> 2) * 80 + (tv & 3) * 16) = xv;
  if (tv < 128) *(u32x4*)(base + K2_KC + (64 + (tv >> 2)) * 80 + (tv & 3) * 16) = z4;
  int unit = tv + 4;
#pragma unroll 1
  for (int rep = 0; rep < 2; ++rep) {
    const unsigned wd[8] = {cur[0], cur[1], cur[2], cur[3], nxt[0], nxt[1], nxt[2], nxt[3]};
#pragma unroll
    for (int sh = 1; sh <= 8; ++sh) {
      u32x4 o;
      const int a = sh >> 1;
#pragma unroll
      for (int i = 0; i < 4; ++i) o[i] = (sh & 1) ? __builtin_amdgcn_alignbit(wd[a + i + 1], wd[a + i], 16) : wd[a + i];
      *(u32x4*)(base + (sh - 1) * K2_COPY + unit * 16) = o;
    }
    if (tv >= 4) break;
    nxt = (tv == 0) ? cur : z4;
    cur = z4;
    unit = (tv + 3) & 3;
  }
  STAMP(1,1);
  __syncthreads();
  STAMP(1,2);
  const unsigned char* kcb = base + il * K2_COPY + (2 + ih + hh + 32 * qq) * 16;
  const unsigned char* xb = base + K2_KC + (63 - col + 8 * qq) * 80 + (16 + 8 * hh) * 2;
  f32x16 acc0, acc1;
#pragma unroll
  for (int i = 0; i < 16; ++i) { acc0[i] = 0.f; acc1[i] = 0.f; }
#pragma unroll 2
  for (int c = 0; c < 8; ++c) {
#pragma unroll
    for (int s = 0; s < 2; ++s) {
      const bf16x8 a0 = *(const bf16x8*)(kcb + (4 * c - 2 * s) * 16);
      const bf16x8 a1 = *(const bf16x8*)(kcb + (4 * c - 2 * s + 128) * 16);
      const bf16x8 b0 = *(const bf16x8*)(xb + c * 80 - 32 * s);
      const bf16x8 b1 = *(const bf16x8*)(xb + (c - 32) * 80 - 32 * s);
      acc0 = __builtin_amdgcn_mfma_f32_32x32x16_bf16(a0, b0, acc0, 0, 0, 0);
      acc1 = __builtin_amdgcn_mfma_f32_32x32x16_bf16(a0, b1, acc1, 0, 0, 0);
      acc1 = __builtin_amdgcn_mfma_f32_32x32x16_bf16(a1, b0, acc1, 0, 0, 0);
    }
  }
  STAMP(1,3);
  __syncthreads();
  f32x4* red = (f32x4*)base;
#pragma unroll
  for (int g = 0; g < 4; ++g) {
    red[(qq * 8 + g) * 64 + lane] = (f32x4){acc0[4 * g], acc0[4 * g + 1], acc0[4 * g + 2], acc0[4 * g + 3]};
    red[(qq * 8 + 4 + g) * 64 + lane] = (f32x4){acc1[4 * g], acc1[4 * g + 1], acc1[4 * g + 2], acc1[4 * g + 3]};
  }
  __syncthreads();
  STAMP(1,4);
  unsigned pk[2][2];
#pragma unroll
  for (int e = 0; e < 2; ++e) {
    const f32x4* rp = red + (2 * qq + e) * 64 + lane;
    const f32x4 y4 = (rp[0] + rp[512]) + (rp[1024] + rp[1536]);
    const int t0 = 2044 - (nb0 + 8 * e);
    const u32x2 kvp = *(const u32x2*)(base + K2_KC + (t0 >> 5) * 80 + (t0 & 31) * 2);
#pragma unroll
    for (int r = 0; r < 2; ++r) {
      const f32x2 kvf = {bfhi(kvp[1 - r]), bflo(kvp[1 - r])};
      const f32x2 xf = unpk(xq[e][r]);
      const f32x2 yy = rbf2((f32x2){y4[2 * r], y4[2 * r + 1]});
      const f32x2 y2 = rbf2(yy + rbf2(kvf * Dd));
      const f32x2 qn = rbf2(rbf2(xf - (f32x2){mm[e][2 * r], mm[e][2 * r + 1]}) * (f32x2){dd[e][2 * r], dd[e][2 * r + 1]});
      pk[e][r] = pk2(xf + rbf2(qn * y2));
    }
  }
  auto s0 = __builtin_amdgcn_permlane32_swap(pk[0][0], pk[1][0], false, false);
  auto s1 = __builtin_amdgcn_permlane32_swap(pk[0][1], pk[1][1], false, false);
  const u32x4 ov = {s0[0], s1[0], s0[1], s1[1]};
  STAMP(1,5);
  *(u32x4*)(hT + d * S_ + 1024 * (qq >> 1) + 32 * col + 16 * (qq & 1) + 8 * hh) = ov;
  STAMP(1,6);
  DRAIN();
  STAMP(1,7);
}

DEV void gload16(bf16x8& dst, const void* sbase, unsigned voff) {
  asm volatile("global_load_dwordx4 %0, %1, %2" : "=v"(dst) : "v"(voff), "s"(sbase) : "memory");
}

template <bool STATS>
DEV void k3_main(lds_u8* smem, const u16* wp, int ft, int w, int lane, float* stl, f32x16* acc) {
  const int hf = lane >> 5, q4 = (lane & 15) >> 2;
  {
    const int bsl = 2 * ((lane >> 4) & 1) + ((lane & 3) >> 1);
    int boff[4];
#pragma unroll
    for (int i = 0; i < 4; ++i)
      boff[i] = (8 * hf + q4) * 256 + (((4 * ((i + w) & 3) + bsl) ^ (q4 << 2)) * 16) + (lane & 1) * 8;
    const char* wbase = (const char*)(wp + ((size_t)ft * 7 + w) * 32 * 512);
    const unsigned voffW = lane * 16;
    bf16x8 wf[32];
#pragma unroll
    for (int i = 0; i < 12; ++i) gload16(wf[i], wbase + i * 1024, voffW);
#pragma unroll
    for (int j = 0; j < 4; ++j)
#pragma unroll
      for (int i = 0; i < 16; ++i) acc[j][i] = 0.f;
    float sum0 = 0.f, sum1 = 0.f, sq0 = 0.f, sq1 = 0.f;
    bf16x8 b0[4], b1[4];
    auto ld = [&](int kg, bf16x8* bb) {
#pragma unroll
      for (int i = 0; i < 4; ++i) bb[i] = tr_frag(smem + kg * 4096 + boff[i], 1024);
    };
    auto mm = [&](int kg, const bf16x8* bb) {
#pragma unroll
      for (int i = 0; i < 4; ++i) acc[i] = __builtin_amdgcn_mfma_f32_32x32x16_bf16(wf[kg], bb[i], acc[i], 0, 0, 0);
      if (STATS) {
        const u32x4 bu = __builtin_bit_cast(u32x4, bb[0]);
#pragma unroll
        for (int i = 0; i < 4; ++i) {
          const unsigned xi = bu[i];
          const float lo = bflo(xi), hi = bfhi(xi);
          sum0 += lo; sum1 += hi;
          sq0 = fmaf(lo, lo, sq0); sq1 = fmaf(hi, hi, sq1);
        }
        asm volatile("" : "+v"(sum0), "+v"(sum1), "+v"(sq0), "+v"(sq1));
      }
    };
    STAMP(2,1);
    asm volatile("s_waitcnt vmcnt(8)" : "+v"(wf[0]), "+v"(wf[1]), "+v"(wf[2]), "+v"(wf[3]) :: "memory");
    __builtin_amdgcn_s_barrier();
    STAMP(2,2);
    ld(0, b0);
#pragma unroll
    for (int c = 0; c < 8; ++c) {
      if (c < 5) {
#pragma unroll
        for (int i = 0; i < 4; ++i) gload16(wf[4 * c + 12 + i], wbase + (4 * c + 12 + i) * 1024, voffW);
      }
      ld(4 * c + 1, b1); mm(4 * c, b0);
      ld(4 * c + 2, b0); mm(4 * c + 1, b1);
      ld(4 * c + 3, b1); mm(4 * c + 2, b0);
      if (c < 7) {
        asm volatile("s_waitcnt vmcnt(%4)" : "+v"(wf[4 * c + 4]), "+v"(wf[4 * c + 5]), "+v"(wf[4 * c + 6]), "+v"(wf[4 * c + 7])
                     : "i"(c < 5 ? 8 : (c == 5 ? 4 : 0)) : "memory");
        __builtin_amdgcn_s_barrier();
        if (c == 0) STAMP(2,3);
        if (c == 3) STAMP(2,4);
        ld(4 * c + 4, b0);
      }
      mm(4 * c + 3, b1);
    }
    STAMP(2,5);
    if (STATS) {
      float sum = sum0 + sum1, sq = sq0 + sq1;
      sum += __shfl_xor(sum, 32);
      sq += __shfl_xor(sq, 32);
      const float mean = sum * (1.0f / D_);
      const float var = rbf(fmaxf(sq * (1.0f / D_) - mean * mean, 0.f));
      const float inv_ = 1.0f / rbf(sqrtf(rbf(var + 1e-5f)));
      if (lane < 32) *(f32x2*)(stl + (w * 32 + lane) * 2) = (f32x2){inv_, -rbf(mean) * inv_};
    }
  }
}

__global__ __launch_bounds__(512) void k3_gemm1(const u16* __restrict__ hT, const u16* __restrict__ wp,
                                                const float* __restrict__ csp, u16* __restrict__ hh) {
  __shared__ __attribute__((aligned(16))) unsigned char smem_[131072 + 2048 + 7168];
  lds_u8* smem = (lds_u8*)smem_;
  const unsigned sbase = (unsigned)(size_t)smem;
  const int t = threadIdx.x, lane = t & 63;
  const int w = __builtin_amdgcn_readfirstlane(t >> 6);
  const int b = blockIdx.x, x = b & 7, idx = b >> 3;
  const int mt = (x & 1) * 8 + (idx & 7), ft = (x >> 1) * 4 + (idx >> 3);
  const int m0 = mt * 128, f0 = ft * 112;
  STAMP(2,0);
  float* csl = (float*)(smem_ + 131072);
  float* stl = csl + 224;
  const int hf = lane >> 5;
  f32x16 acc[4];
  if (w < 4) {
    k3_main<true>(smem, wp, ft, w, lane, stl, acc);
  } else if (w < 7) {
    k3_main<false>(smem, wp, ft, w, lane, stl, acc);
  } else {
    const unsigned voffA = ((lane >> 4) * S_ + (((lane & 15) ^ ((lane >> 4) << 2)) * 8)) * 2;
    const char* abase = (const char*)(hT + m0);
    auto issue = [&](int c) {
#pragma unroll
      for (int i = 0; i < 16; ++i) glds16(abase + (size_t)(c * 64 + 4 * i) * S_ * 2, voffA, sbase + c * 16384 + i * 1024);
    };
    bf16x8 cpart[8];
    const unsigned voffC = (lane < 56 ? lane : 55) * 16;
#pragma unroll
    for (int k8 = 0; k8 < 8; ++k8) gload16(cpart[k8], (const char*)(csp + (k8 * 16 + ft) * 224), voffC);
    issue(0); issue(1);
#pragma unroll 1
    for (int c = 0; c < 8; ++c) {
      if (c == 0 || c == 6) WAITV(16); else if (c < 6) WAITV(32); else WAITV(0);
      __builtin_amdgcn_s_barrier();
      if (c == 0) issue(2);
      if (c < 5) issue(c + 3);
    }
    asm volatile("" : "+v"(cpart[0]), "+v"(cpart[1]), "+v"(cpart[2]), "+v"(cpart[3]),
                      "+v"(cpart[4]), "+v"(cpart[5]), "+v"(cpart[6]), "+v"(cpart[7]));
    f32x4 cs4 = __builtin_bit_cast(f32x4, cpart[0]);
#pragma unroll
    for (int k8 = 1; k8 < 8; ++k8) cs4 += __builtin_bit_cast(f32x4, cpart[k8]);
    if (lane < 56) *(f32x4*)(csl + lane * 4) = cs4;
  }
  __syncthreads();
  if (w < 7) {
    f32x4 cg[2], cu[2];
#pragma unroll
    for (int g = 0; g < 2; ++g) {
      cg[g] = *(const f32x4*)(csl + 16 * w + 8 * g + 4 * hf);
      cu[g] = *(const f32x4*)(csl + 112 + 16 * w + 8 * g + 4 * hf);
    }
    u16* orow = hh + (size_t)(m0 + (lane & 31)) * F_ + f0 + 16 * w + 8 * hf;
#pragma unroll
    for (int i = 0; i < 4; ++i) {
      const int mb = (i + w) & 3;
      const f32x2 st2 = *(const f32x2*)(stl + (mb * 32 + (lane & 31)) * 2);
      const float inv = st2[0], mi = st2[1];
      unsigned o[4];
#pragma unroll
      for (int g = 0; g < 2; ++g) {
#pragma unroll
        for (int r = 0; r < 2; ++r) {
          const f32x2 ag = {acc[i][4 * g + 2 * r], acc[i][4 * g + 2 * r + 1]};
          const f32x2 au = {acc[i][8 + 4 * g + 2 * r], acc[i][8 + 4 * g + 2 * r + 1]};
          f32x2 gt = ag * inv + (f32x2){cg[g][2 * r], cg[g][2 * r + 1]} * mi;
          const f32x2 up = au * inv + (f32x2){cu[g][2 * r], cu[g][2 * r + 1]} * mi;
          gt[0] = gt[0] > 0.f ? gt[0] : 0.f;
          gt[1] = gt[1] > 0.f ? gt[1] : 0.f;
          o[2 * g + r] = pk2(gt * up);
        }
      }
      auto s0 = __builtin_amdgcn_permlane32_swap(o[0], o[2], false, false);
      auto s1 = __builtin_amdgcn_permlane32_swap(o[1], o[3], false, false);
      u32x4 ov = {s0[0], s1[0], s0[1], s1[1]};
      *(u32x4*)(orow + (size_t)mb * 32 * F_) = ov;
    }
  }
  STAMP(2,6);
  DRAIN();
  STAMP(2,7);
}

static constexpr int K4_STAGE = 32768;

__global__ __launch_bounds__(384) void k4_gemm2(const u16* __restrict__ hh, const u16* __restrict__ wd,
                                                const u16* __restrict__ hT, u16* __restrict__ out) {
  __shared__ __attribute__((aligned(16))) unsigned char smem_[4 * K4_STAGE];
  lds_u8* smem = (lds_u8*)smem_;
  const int t = threadIdx.x, lane = t & 63;
  const int w = __builtin_amdgcn_readfirstlane(t >> 6);
  const int b = blockIdx.x, x = b & 7, idx = b >> 3;
  const int mt = x * 4 + (idx & 3), nt = idx >> 2;
  const int m0 = mt * 64, n0 = nt * 64;
  STAMP(3,0);
  f32x16 acc[2][2];
  u32x4 hr0, hr1;
  const int hf = lane >> 5, r32 = lane & 31;
  if (w >= 4) {
    const unsigned sbase = (unsigned)(size_t)smem + (w - 4) * 16384;
    unsigned voff[4];
    const char* base;
    size_t pstride, sstride;
    if (w == 4) {
      const int ar = lane >> 4, as = lane & 15;
#pragma unroll
      for (int i = 0; i < 4; ++i) voff[i] = ((4 * i + ar) * F_ + (as ^ (4 * i + ar)) * 8) * 2;
      base = (const char*)(hh + (size_t)m0 * F_);
      pstride = (size_t)16 * F_ * 2; sstride = 256;
    } else {
      const int br = lane >> 3, bs = lane & 7;
#pragma unroll
      for (int i = 0; i < 4; ++i) voff[i] = ((8 * i + br) * D_ + (bs ^ (((br >> 1) & 1) << 2)) * 8) * 2;
      base = (const char*)(wd + n0);
      pstride = (size_t)32 * D_ * 2; sstride = (size_t)128 * D_ * 2;
    }
    auto issue = [&](int st) {
      const unsigned sb = sbase + (st & 3) * K4_STAGE;
      const char* pp = base + st * sstride;
#pragma unroll
      for (int i = 0; i < 16; ++i) glds16(pp + (i >> 2) * pstride, voff[i & 3], sb + i * 1024);
    };
    issue(0); issue(1);
#pragma unroll 1
    for (int st = 0; st < 14; ++st) {
      if (st == 0 || st == 12) WAITV(16); else if (st < 12) WAITV(32); else WAITV(0);
      __builtin_amdgcn_s_barrier();
      if (st == 0) issue(2);
      if (st + 3 < 14) issue(st + 3);
    }
  } else {
    hr0 = *(const u32x4*)(hT + (n0 + lane) * S_ + m0 + 16 * w);
    hr1 = *(const u32x4*)(hT + (n0 + lane) * S_ + m0 + 16 * w + 8);
#pragma unroll
    for (int i = 0; i < 2; ++i)
#pragma unroll
      for (int j = 0; j < 2; ++j)
#pragma unroll
        for (int e = 0; e < 16; ++e) acc[i][j][e] = 0.f;
    const int bq = (lane & 15) >> 2;
    const int bsw = ((bq >> 1) & 1) << 2;
    const int bcol = 16 * ((lane >> 4) & 1) + 4 * (lane & 3);
    const int boff0 = 16384 + (32 * w + 8 * hf + bq) * 128 + (((bcol >> 3) ^ bsw) * 16) + (bcol & 7) * 2;
    const int boff1 = 16384 + (32 * w + 8 * hf + bq) * 128 + ((((bcol + 32) >> 3) ^ bsw) * 16) + (bcol & 7) * 2;
    const int aoff0 = r32 * 256 + (((4 * w + hf) ^ (r32 & 15)) * 16);
    const int aoff1 = r32 * 256 + (((4 * w + 2 + hf) ^ (r32 & 15)) * 16);
#pragma unroll 1
    for (int st = 0; st < 14; ++st) {
      if (st == 0) STAMP(3,1);
      __builtin_amdgcn_s_barrier();
      if (st == 0) STAMP(3,2);
      if (st == 1) STAMP(3,3);
      if (st == 7) STAMP(3,4);
      const lds_u8* sb = smem + (st & 3) * K4_STAGE;
      bf16x8 af[2][2], bf[2][2];
#pragma unroll
      for (int mb = 0; mb < 2; ++mb) {
        af[0][mb] = *(const __attribute__((address_space(3))) bf16x8*)(sb + aoff0 + mb * 8192);
        af[1][mb] = *(const __attribute__((address_space(3))) bf16x8*)(sb + aoff1 + mb * 8192);
      }
#pragma unroll
      for (int ks = 0; ks < 2; ++ks) {
        bf[ks][0] = tr_frag(sb + boff0 + ks * 16 * 128, 4 * 128);
        bf[ks][1] = tr_frag(sb + boff1 + ks * 16 * 128, 4 * 128);
      }
#pragma unroll
      for (int ks = 0; ks < 2; ++ks)
#pragma unroll
        for (int mb = 0; mb < 2; ++mb)
#pragma unroll
          for (int nb = 0; nb < 2; ++nb)
            acc[mb][nb] = __builtin_amdgcn_mfma_f32_32x32x16_bf16(af[ks][mb], bf[ks][nb], acc[mb][nb], 0, 0, 0);
    }
  }
  STAMP(3,5);
  __syncthreads();
  float* red = (float*)smem_;
  if (w < 4) {
#pragma unroll
    for (int mb = 0; mb < 2; ++mb)
#pragma unroll
      for (int nb = 0; nb < 2; ++nb)
#pragma unroll
        for (int g = 0; g < 4; ++g)
          *(f32x4*)(red + w * 4352 + (32 * nb + r32) * 68 + 32 * mb + 8 * g + 4 * hf) =
              (f32x4){acc[mb][nb][4 * g], acc[mb][nb][4 * g + 1], acc[mb][nb][4 * g + 2], acc[mb][nb][4 * g + 3]};
  }
  __syncthreads();
  u16* ob = (u16*)(smem_ + 71680);
  if (w < 4) {
#pragma unroll
    for (int i = 0; i < 4; ++i) {
      const float* rp = red + lane * 68 + 16 * w + 4 * i;
      const f32x4 s = (*(const f32x4*)(rp) + *(const f32x4*)(rp + 4352)) + (*(const f32x4*)(rp + 8704) + *(const f32x4*)(rp + 13056));
      const unsigned h0 = i < 2 ? hr0[2 * i] : hr1[2 * i - 4];
      const unsigned h1 = i < 2 ? hr0[2 * i + 1] : hr1[2 * i - 3];
      const unsigned o0 = pk2(unpk(h0) + rbf2((f32x2){s[0], s[1]}));
      const unsigned o1 = pk2(unpk(h1) + rbf2((f32x2){s[2], s[3]}));
      u16* op = ob + (16 * w + 4 * i) * 72 + lane;
      op[0] = (u16)o0; op[72] = (u16)(o0 >> 16); op[144] = (u16)o1; op[216] = (u16)(o1 >> 16);
    }
  }
  __syncthreads();
  if (w < 4) {
#pragma unroll
    for (int i = 0; i < 2; ++i) {
      const int p = t + 256 * i, row = p >> 3, c = p & 7;
      *(u32x4*)(out + (m0 + row) * D_ + n0 + c * 8) = *(const u32x4*)(ob + row * 72 + c * 8);
    }
  }
  STAMP(3,6);
  DRAIN();
  STAMP(3,7);
}

extern "C" void kernel_launch(void* const* d_in, const int* in_sizes, int n_in, void* d_out, int out_size,
                              void* d_ws, size_t ws_size, hipStream_t stream) {
  const u16* q = (const u16*)d_in[0];
  const u16* k = (const u16*)d_in[1];
  const u16* v = (const u16*)d_in[2];
  const u16* shk = (const u16*)d_in[3];
  const u16* ssmk = (const u16*)d_in[4];
  const u16* ssmD = (const u16*)d_in[5];
  const u16* wg = (const u16*)d_in[7];
  const u16* wu = (const u16*)d_in[8];
  const u16* wd = (const u16*)d_in[9];
  unsigned char* ws = (unsigned char*)d_ws;
  u16* kvR = (u16*)(ws);
  u16* xT = (u16*)(ws + (2u << 20));
  float* qm = (float*)(ws + (4u << 20));
  float* qinv = qm + 2048;
  float* csp = (float*)(ws + (4u << 20) + 65536);
  u16* h = (u16*)(ws + (5u << 20));
  u16* hh = (u16*)(ws + (8u << 20));
  u16* wp = (u16*)(ws + (16u << 20));
  k1_prep<<<384, 512, 0, stream>>>(q, k, v, shk, kvR, xT, qm, qinv);
  k2_conv<<<412, 512, 0, stream>>>(ssmk, ssmD, kvR, xT, qm, qinv, h, wg, wu, wp, csp, wd, (unsigned*)(ws + (24u << 20)));
  k3_gemm1<<<256, 512, 0, stream>>>(h, wp, csp, hh);
  k4_gemm2<<<256, 384, 0, stream>>>(hh, wd, h, (u16*)d_out);
}
```

```cpp
#include <hip/hip_runtime.h>
#include <hip/hip_bf16.h>
#include <stdint.h>

typedef unsigned short u16;
typedef __attribute__((ext_vector_type(8))) short bf16x8;
typedef __attribute__((ext_vector_type(4))) short s16x4;
typedef __attribute__((ext_vector_type(16))) float f32x16;
typedef __attribute__((ext_vector_type(4))) float f32x4;
typedef __attribute__((ext_vector_type(2))) float f32x2;
typedef __attribute__((ext_vector_type(4))) unsigned u32x4;
typedef __attribute__((ext_vector_type(2))) unsigned u32x2;
typedef __attribute__((ext_vector_type(2))) __bf16 bf16x2_t;
typedef __attribute__((ext_vector_type(4))) short v4i16_t;
typedef __attribute__((address_space(3))) unsigned char lds_u8;

#define DEV __device__ __forceinline__
#define STAMP(kid,i)
#define DRAIN()

static constexpr int S_ = 2048, D_ = 512, F_ = 1792;

DEV float bflo(unsigned x) { return __uint_as_float(x << 16); }
DEV float bfhi(unsigned x) { return __uint_as_float(x & 0xffff0000u); }
DEV f32x2 unpk(unsigned x) { return (f32x2){bflo(x), bfhi(x)}; }
DEV unsigned pk2(f32x2 v) {
  bf16x2_t r = {(__bf16)v[0], (__bf16)v[1]};
  return __builtin_bit_cast(unsigned, r);
}
DEV f32x2 rbf2(f32x2 v) { return unpk(pk2(v)); }
DEV float rbf(float x) { return (float)(__bf16)x; }

DEV void glds16(const void* sbase, unsigned voff, unsigned lds_dst) {
  asm volatile("s_mov_b32 m0, %2\n\ts_nop 0\n\tglobal_load_lds_dwordx4 %0, %1"
               :: "v"(voff), "s"(sbase), "s"(lds_dst) : "memory");
}
#define WAITV(n) asm volatile("s_waitcnt vmcnt(" #n ")" ::: "memory")

DEV s16x4 tr_read(const lds_u8* p) {
  return __builtin_bit_cast(s16x4, __builtin_amdgcn_ds_read_tr16_b64_v4i16((__attribute__((address_space(3))) v4i16_t*)p));
}
DEV bf16x8 tr_frag(const lds_u8* p, int rowstride4) {
  const s16x4 lo = tr_read(p);
  const s16x4 hi = tr_read(p + rowstride4);
  return (bf16x8){lo[0], lo[1], lo[2], lo[3], hi[0], hi[1], hi[2], hi[3]};
}

__global__ __launch_bounds__(512) void k1_prep(const u16* __restrict__ q, const u16* __restrict__ k,
                                               const u16* __restrict__ v, const u16* __restrict__ shk,
                                               const u16* __restrict__ wg, const u16* __restrict__ wu,
                                               u16* __restrict__ kvR, u16* __restrict__ xT,
                                               float* __restrict__ qm, float* __restrict__ qinv,
                                               u16* __restrict__ wp, float* __restrict__ csp,
                                               const u16* __restrict__ ssmk, const u16* __restrict__ wd,
                                               unsigned* __restrict__ sink) {
  __shared__ __attribute__((aligned(16))) unsigned char smem[28672 + 14336];
  const int b = blockIdx.x, t = threadIdx.x;
  if (b >= 512) {
    const int pb = b - 512;
    const u32x4* src = pb < 32 ? (const u32x4*)ssmk + pb * 4096 : (const u32x4*)wd + (pb - 32) * 4096;
    unsigned a = 0;
#pragma unroll
    for (int i = 0; i < 8; ++i) { const u32x4 x = src[i * 512 + t]; a |= x[0] ^ x[1] ^ x[2] ^ x[3]; }
    if (a == 0x9e3779b9u) sink[t] = a;
  } else if (b < 256) {
    u16* kvs = (u16*)smem;
    u16* xs = (u16*)(smem + 64 * 66 * 2);
    const int s0 = (b & 31) * 64, d0 = (b >> 5) * 64;
    const int c = t & 7, r = t >> 3, s = s0 + r;
    u32x4 wv[4], kr[4];
#pragma unroll
    for (int i = 0; i < 4; ++i) wv[i] = *(const u32x4*)(shk + (d0 + 8 * c) * 4 + 8 * i);
#pragma unroll
    for (int j = 0; j < 4; ++j) {
      const int ss = s - 3 + j;
      u32x4 z = {0u, 0u, 0u, 0u};
      kr[j] = (ss >= 0) ? *(const u32x4*)(k + ss * D_ + d0 + 8 * c) : z;
    }
    const u32x4 vv = *(const u32x4*)(v + s * D_ + d0 + 8 * c);
    const u32x4 qq = *(const u32x4*)(q + s * D_ + d0 + 8 * c);
#pragma unroll
    for (int p = 0; p < 4; ++p) {
      const f32x2 w0 = {bflo(wv[p][0]), bflo(wv[p][2])}, w1 = {bfhi(wv[p][0]), bfhi(wv[p][2])};
      const f32x2 w2 = {bflo(wv[p][1]), bflo(wv[p][3])}, w3 = {bfhi(wv[p][1]), bfhi(wv[p][3])};
      f32x2 ks = rbf2(unpk(kr[0][p]) * w3);
      ks = rbf2(ks + rbf2(unpk(kr[1][p]) * w2));
      ks = rbf2(ks + rbf2(unpk(kr[2][p]) * w1));
      ks = rbf2(ks + rbf2(unpk(kr[3][p]) * w0));
      const unsigned kvb = pk2(ks * unpk(vv[p]));
      kvs[(8 * c + 2 * p) * 66 + (63 - r)] = (u16)kvb;
      kvs[(8 * c + 2 * p + 1) * 66 + (63 - r)] = (u16)(kvb >> 16);
      xs[(8 * c + 2 * p) * 66 + r] = (u16)qq[p];
      xs[(8 * c + 2 * p + 1) * 66 + r] = (u16)(qq[p] >> 16);
    }
    __syncthreads();
    {
      const int ch = r;
      const unsigned* a = (const unsigned*)(kvs + ch * 66 + 8 * c);
      const unsigned* bq = (const unsigned*)(xs + ch * 66 + 8 * c);
      u32x4 o1 = {a[0], a[1], a[2], a[3]};
      u32x4 o2 = {bq[0], bq[1], bq[2], bq[3]};
      *(u32x4*)(kvR + (d0 + ch) * S_ + (1984 - s0) + 8 * c) = o1;
      *(u32x4*)(xT + (d0 + ch) * S_ + s0 + 8 * c) = o2;
    }
  } else if (b < 384) {
    const int lane = t & 63, w = t >> 6;
#pragma unroll
    for (int i = 0; i < 2; ++i) {
      const int row = (b - 256) * 16 + w * 2 + i;
      const u32x4 x = *(const u32x4*)(q + row * D_ + lane * 8);
      f32x2 f[4];
#pragma unroll
      for (int j = 0; j < 4; ++j) f[j] = unpk(x[j]);
      f32x2 s2 = (f[0] + f[1]) + (f[2] + f[3]);
      float sum = s2[0] + s2[1];
#pragma unroll
      for (int o = 32; o >= 1; o >>= 1) sum += __shfl_xor(sum, o);
      const float mean = sum * (1.0f / D_);
      f32x2 q2 = {0.f, 0.f};
#pragma unroll
      for (int j = 0; j < 4; ++j) { const f32x2 d = f[j] - mean; q2 += d * d; }
      float sq = q2[0] + q2[1];
#pragma unroll
      for (int o = 32; o >= 1; o >>= 1) sq += __shfl_xor(sq, o);
      const float var = rbf(sq * (1.0f / D_));
      if (lane == 0) {
        qm[row] = rbf(mean);
        qinv[row] = 1.0f / rbf(sqrtf(rbf(var + 1e-5f)));
      }
    }
  } else {
    lds_u8* tile = (lds_u8*)smem;
    float* red = (float*)(smem + 28672);
    const int bb = b - 384, ft = bb & 15, kc = bb >> 4;
    const int part = t % 28, rg = t / 28;
    if (rg < 16) {
      f32x2 acc[4] = {{0.f, 0.f}, {0.f, 0.f}, {0.f, 0.f}, {0.f, 0.f}};
      const int mat = part >= 14;
      const u16* src = (mat ? wu : wg) + ft * 112 + (part - 14 * mat) * 8;
#pragma unroll
      for (int i = 0; i < 4; ++i) {
        const int kk = rg * 4 + i;
        const u32x4 x = *(const u32x4*)(src + (kc * 64 + kk) * F_);
        *(u32x4*)(smem + kk * 448 + part * 16) = x;
#pragma unroll
        for (int j = 0; j < 4; ++j) acc[j] += unpk(x[j]);
      }
      *(f32x4*)(red + rg * 224 + part * 8) = (f32x4){acc[0][0], acc[0][1], acc[1][0], acc[1][1]};
      *(f32x4*)(red + rg * 224 + part * 8 + 4) = (f32x4){acc[2][0], acc[2][1], acc[3][0], acc[3][1]};
    }
    __syncthreads();
    const int lane = t & 63, w = t >> 6;
    const int woff = (8 * (lane >> 5) + ((lane & 15) >> 2)) * 448 + ((lane >> 4) & 1) * 224 + (lane & 3) * 8;
#pragma unroll 1
    for (int f = w; f < 28; f += 8) {
      const int j = f >> 2, ks = f & 3;
      const bf16x8 fr = tr_frag(tile + woff + ks * 16 * 448 + j * 32, 4 * 448);
      *(bf16x8*)(wp + ((((size_t)ft * 7 + j) * 32 + kc * 4 + ks) * 64 + lane) * 8) = fr;
    }
    if (t < 224) {
      float s = 0.f;
#pragma unroll
      for (int i = 0; i < 16; ++i) s += red[i * 224 + t];
      csp[(kc * 16 + ft) * 224 + t] = s;
    }
  }
}

static constexpr int K2_COPY = 4160;
static constexpr int K2_KC = 8 * K2_COPY;
static constexpr int K2_XR = 96 * 80;
static constexpr int K2_CH = K2_KC + K2_XR;

__global__ __launch_bounds__(512) void k2_conv(const u16* __restrict__ ssmk, const u16* __restrict__ ssmD,
                                               const u16* __restrict__ kvR, const u16* __restrict__ xT,
                                               const float* __restrict__ qm, const float* __restrict__ qinv,
                                               u16* __restrict__ hT) {
  __shared__ __attribute__((aligned(16))) unsigned char smem[2 * K2_CH];
  const int t = threadIdx.x, lane = t & 63, w = t >> 6;
  const int d0 = blockIdx.x * 2;
  const u32x4 z4 = {0u, 0u, 0u, 0u};
  const int ch = w >> 2, qq = w & 3;
  const int il = lane & 7, ih = (lane >> 3) & 3, hh = lane >> 5, col = lane & 31;
  const int d = d0 + ch;
  STAMP(1,0);
  const int tv = t & 255;
  const u16* kk = ssmk + d * S_;
  u32x4 cur = *(const u32x4*)(kk + tv * 8);
  u32x4 nxt = (tv < 255) ? *(const u32x4*)(kk + tv * 8 + 8) : z4;
  const u32x4 xv = *(const u32x4*)(kvR + d * S_ + tv * 8);
  const int nb0 = 1024 * (qq >> 1) + 32 * col + 16 * (qq & 1) + 4 * hh;
  u32x2 xq[2];
  f32x4 mm[2], dd[2];
#pragma unroll
  for (int e = 0; e < 2; ++e) {
    xq[e] = *(const u32x2*)(xT + d * S_ + nb0 + 8 * e);
    mm[e] = *(const f32x4*)(qm + nb0 + 8 * e);
    dd[e] = *(const f32x4*)(qinv + nb0 + 8 * e);
  }
  const float Dd = bflo(ssmD[d]);
  unsigned char* base = smem + ch * K2_CH;
  *(u32x4*)(base + K2_KC + (tv >> 2) * 80 + (tv & 3) * 16) = xv;
  if (tv < 128) *(u32x4*)(base + K2_KC + (64 + (tv >> 2)) * 80 + (tv & 3) * 16) = z4;
  int unit = tv + 4;
#pragma unroll 1
  for (int rep = 0; rep < 2; ++rep) {
    const unsigned wd[8] = {cur[0], cur[1], cur[2], cur[3], nxt[0], nxt[1], nxt[2], nxt[3]};
#pragma unroll
    for (int sh = 1; sh <= 8; ++sh) {
      u32x4 o;
      const int a = sh >> 1;
#pragma unroll
      for (int i = 0; i < 4; ++i) o[i] = (sh & 1) ? __builtin_amdgcn_alignbit(wd[a + i + 1], wd[a + i], 16) : wd[a + i];
      *(u32x4*)(base + (sh - 1) * K2_COPY + unit * 16) = o;
    }
    if (tv >= 4) break;
    nxt = (tv == 0) ? cur : z4;
    cur = z4;
    unit = (tv + 3) & 3;
  }
  STAMP(1,1);
  __syncthreads();
  STAMP(1,2);
  const unsigned char* kcb = base + il * K2_COPY + (2 + ih + hh + 32 * qq) * 16;
  const unsigned char* xb = base + K2_KC + (63 - col + 8 * qq) * 80 + (16 + 8 * hh) * 2;
  f32x16 acc0, acc1;
#pragma unroll
  for (int i = 0; i < 16; ++i) { acc0[i] = 0.f; acc1[i] = 0.f; }
#pragma unroll 2
  for (int c = 0; c < 8; ++c) {
#pragma unroll
    for (int s = 0; s < 2; ++s) {
      const bf16x8 a0 = *(const bf16x8*)(kcb + (4 * c - 2 * s) * 16);
      const bf16x8 a1 = *(const bf16x8*)(kcb + (4 * c - 2 * s + 128) * 16);
      const bf16x8 b0 = *(const bf16x8*)(xb + c * 80 - 32 * s);
      const bf16x8 b1 = *(const bf16x8*)(xb + (c - 32) * 80 - 32 * s);
      acc0 = __builtin_amdgcn_mfma_f32_32x32x16_bf16(a0, b0, acc0, 0, 0, 0);
      acc1 = __builtin_amdgcn_mfma_f32_32x32x16_bf16(a0, b1, acc1, 0, 0, 0);
      acc1 = __builtin_amdgcn_mfma_f32_32x32x16_bf16(a1, b0, acc1, 0, 0, 0);
    }
  }
  STAMP(1,3);
  __syncthreads();
  f32x4* red = (f32x4*)base;
#pragma unroll
  for (int g = 0; g < 4; ++g) {
    red[(qq * 8 + g) * 64 + lane] = (f32x4){acc0[4 * g], acc0[4 * g + 1], acc0[4 * g + 2], acc0[4 * g + 3]};
    red[(qq * 8 + 4 + g) * 64 + lane] = (f32x4){acc1[4 * g], acc1[4 * g + 1], acc1[4 * g + 2], acc1[4 * g + 3]};
  }
  __syncthreads();
  STAMP(1,4);
  unsigned pk[2][2];
#pragma unroll
  for (int e = 0; e < 2; ++e) {
    const f32x4* rp = red + (2 * qq + e) * 64 + lane;
    const f32x4 y4 = (rp[0] + rp[512]) + (rp[1024] + rp[1536]);
    const int t0 = 2044 - (nb0 + 8 * e);
    const u32x2 kvp = *(const u32x2*)(base + K2_KC + (t0 >> 5) * 80 + (t0 & 31) * 2);
#pragma unroll
    for (int r = 0; r < 2; ++r) {
      const f32x2 kvf = {bfhi(kvp[1 - r]), bflo(kvp[1 - r])};
      const f32x2 xf = unpk(xq[e][r]);
      const f32x2 yy = rbf2((f32x2){y4[2 * r], y4[2 * r + 1]});
      const f32x2 y2 = rbf2(yy + rbf2(kvf * Dd));
      const f32x2 qn = rbf2(rbf2(xf - (f32x2){mm[e][2 * r], mm[e][2 * r + 1]}) * (f32x2){dd[e][2 * r], dd[e][2 * r + 1]});
      pk[e][r] = pk2(xf + rbf2(qn * y2));
    }
  }
  auto s0 = __builtin_amdgcn_permlane32_swap(pk[0][0], pk[1][0], false, false);
  auto s1 = __builtin_amdgcn_permlane32_swap(pk[0][1], pk[1][1], false, false);
  const u32x4 ov = {s0[0], s1[0], s0[1], s1[1]};
  STAMP(1,5);
  *(u32x4*)(hT + d * S_ + 1024 * (qq >> 1) + 32 * col + 16 * (qq & 1) + 8 * hh) = ov;
  STAMP(1,6);
  DRAIN();
  STAMP(1,7);
}

DEV void gload16(bf16x8& dst, const void* sbase, unsigned voff) {
  asm volatile("global_load_dwordx4 %0, %1, %2" : "=v"(dst) : "v"(voff), "s"(sbase) : "memory");
}

template <bool STATS>
DEV void k3_main(lds_u8* smem, const u16* wp, int ft, int w, int lane, float* stl, f32x16* acc) {
  const int hf = lane >> 5, q4 = (lane & 15) >> 2;
  {
    const int bsl = 2 * ((lane >> 4) & 1) + ((lane & 3) >> 1);
    int boff[4];
#pragma unroll
    for (int i = 0; i < 4; ++i)
      boff[i] = (8 * hf + q4) * 256 + (((4 * ((i + w) & 3) + bsl) ^ (q4 << 2)) * 16) + (lane & 1) * 8;
    const char* wbase = (const char*)(wp + ((size_t)ft * 7 + w) * 32 * 512);
    const unsigned voffW = lane * 16;
    bf16x8 wf[32];
#pragma unroll
    for (int i = 0; i < 12; ++i) gload16(wf[i], wbase + i * 1024, voffW);
#pragma unroll
    for (int j = 0; j < 4; ++j)
#pragma unroll
      for (int i = 0; i < 16; ++i) acc[j][i] = 0.f;
    float sum0 = 0.f, sum1 = 0.f, sq0 = 0.f, sq1 = 0.f;
    bf16x8 b0[4], b1[4];
    auto ld = [&](int kg, bf16x8* bb) {
#pragma unroll
      for (int i = 0; i < 4; ++i) bb[i] = tr_frag(smem + kg * 4096 + boff[i], 1024);
    };
    auto mm = [&](int kg, const bf16x8* bb) {
#pragma unroll
      for (int i = 0; i < 4; ++i) acc[i] = __builtin_amdgcn_mfma_f32_32x32x16_bf16(wf[kg], bb[i], acc[i], 0, 0, 0);
      if (STATS) {
        const u32x4 bu = __builtin_bit_cast(u32x4, bb[0]);
#pragma unroll
        for (int i = 0; i < 4; ++i) {
          const unsigned xi = bu[i];
          const float lo = bflo(xi), hi = bfhi(xi);
          sum0 += lo; sum1 += hi;
          sq0 = fmaf(lo, lo, sq0); sq1 = fmaf(hi, hi, sq1);
        }
        asm volatile("" : "+v"(sum0), "+v"(sum1), "+v"(sq0), "+v"(sq1));
      }
    };
    STAMP(2,1);
    asm volatile("s_waitcnt vmcnt(8)" : "+v"(wf[0]), "+v"(wf[1]), "+v"(wf[2]), "+v"(wf[3]) :: "memory");
    __builtin_amdgcn_s_barrier();
    STAMP(2,2);
    ld(0, b0);
#pragma unroll
    for (int c = 0; c < 8; ++c) {
      if (c < 5) {
#pragma unroll
        for (int i = 0; i < 4; ++i) gload16(wf[4 * c + 12 + i], wbase + (4 * c + 12 + i) * 1024, voffW);
      }
      ld(4 * c + 1, b1); mm(4 * c, b0);
      ld(4 * c + 2, b0); mm(4 * c + 1, b1);
      ld(4 * c + 3, b1); mm(4 * c + 2, b0);
      if (c < 7) {
        asm volatile("s_waitcnt vmcnt(%4)" : "+v"(wf[4 * c + 4]), "+v"(wf[4 * c + 5]), "+v"(wf[4 * c + 6]), "+v"(wf[4 * c + 7])
                     : "i"(c < 5 ? 8 : (c == 5 ? 4 : 0)) : "memory");
        __builtin_amdgcn_s_barrier();
        if (c == 0) STAMP(2,3);
        if (c == 3) STAMP(2,4);
        ld(4 * c + 4, b0);
      }
      mm(4 * c + 3, b1);
    }
    STAMP(2,5);
    if (STATS) {
      float sum = sum0 + sum1, sq = sq0 + sq1;
      sum += __shfl_xor(sum, 32);
      sq += __shfl_xor(sq, 32);
      const float mean = sum * (1.0f / D_);
      const float var = rbf(fmaxf(sq * (1.0f / D_) - mean * mean, 0.f));
      const float inv_ = 1.0f / rbf(sqrtf(rbf(var + 1e-5f)));
      if (lane < 32) *(f32x2*)(stl + (w * 32 + lane) * 2) = (f32x2){inv_, -rbf(mean) * inv_};
    }
  }
}

__global__ __launch_bounds__(512) void k3_gemm1(const u16* __restrict__ hT, const u16* __restrict__ wp,
                                                const float* __restrict__ csp, u16* __restrict__ hh) {
  __shared__ __attribute__((aligned(16))) unsigned char smem_[131072 + 2048 + 7168];
  lds_u8* smem = (lds_u8*)smem_;
  const unsigned sbase = (unsigned)(size_t)smem;
  const int t = threadIdx.x, lane = t & 63;
  const int w = __builtin_amdgcn_readfirstlane(t >> 6);
  const int b = blockIdx.x, x = b & 7, idx = b >> 3;
  const int mt = (x & 1) * 8 + (idx & 7), ft = (x >> 1) * 4 + (idx >> 3);
  const int m0 = mt * 128, f0 = ft * 112;
  STAMP(2,0);
  float* csl = (float*)(smem_ + 131072);
  float* stl = csl + 224;
  const int hf = lane >> 5;
  f32x16 acc[4];
  if (w < 4) {
    k3_main<true>(smem, wp, ft, w, lane, stl, acc);
  } else if (w < 7) {
    k3_main<false>(smem, wp, ft, w, lane, stl, acc);
  } else {
    const unsigned voffA = ((lane >> 4) * S_ + (((lane & 15) ^ ((lane >> 4) << 2)) * 8)) * 2;
    const char* abase = (const char*)(hT + m0);
    auto issue = [&](int c) {
#pragma unroll
      for (int i = 0; i < 16; ++i) glds16(abase + (size_t)(c * 64 + 4 * i) * S_ * 2, voffA, sbase + c * 16384 + i * 1024);
    };
    bf16x8 cpart[8];
    const unsigned voffC = (lane < 56 ? lane : 55) * 16;
#pragma unroll
    for (int k8 = 0; k8 < 8; ++k8) gload16(cpart[k8], (const char*)(csp + (k8 * 16 + ft) * 224), voffC);
    issue(0); issue(1);
#pragma unroll 1
    for (int c = 0; c < 8; ++c) {
      if (c == 0 || c == 6) WAITV(16); else if (c < 6) WAITV(32); else WAITV(0);
      __builtin_amdgcn_s_barrier();
      if (c == 0) issue(2);
      if (c < 5) issue(c + 3);
    }
    asm volatile("" : "+v"(cpart[0]), "+v"(cpart[1]), "+v"(cpart[2]), "+v"(cpart[3]),
                      "+v"(cpart[4]), "+v"(cpart[5]), "+v"(cpart[6]), "+v"(cpart[7]));
    f32x4 cs4 = __builtin_bit_cast(f32x4, cpart[0]);
#pragma unroll
    for (int k8 = 1; k8 < 8; ++k8) cs4 += __builtin_bit_cast(f32x4, cpart[k8]);
    if (lane < 56) *(f32x4*)(csl + lane * 4) = cs4;
  }
  __syncthreads();
  if (w < 7) {
    f32x4 cg[2], cu[2];
#pragma unroll
    for (int g = 0; g < 2; ++g) {
      cg[g] = *(const f32x4*)(csl + 16 * w + 8 * g + 4 * hf);
      cu[g] = *(const f32x4*)(csl + 112 + 16 * w + 8 * g + 4 * hf);
    }
    u16* orow = hh + (size_t)(m0 + (lane & 31)) * F_ + f0 + 16 * w + 8 * hf;
#pragma unroll
    for (int i = 0; i < 4; ++i) {
      const int mb = (i + w) & 3;
      const f32x2 st2 = *(const f32x2*)(stl + (mb * 32 + (lane & 31)) * 2);
      const float inv = st2[0], mi = st2[1];
      unsigned o[4];
#pragma unroll
      for (int g = 0; g < 2; ++g) {
#pragma unroll
        for (int r = 0; r < 2; ++r) {
          const f32x2 ag = {acc[i][4 * g + 2 * r], acc[i][4 * g + 2 * r + 1]};
          const f32x2 au = {acc[i][8 + 4 * g + 2 * r], acc[i][8 + 4 * g + 2 * r + 1]};
          f32x2 gt = ag * inv + (f32x2){cg[g][2 * r], cg[g][2 * r + 1]} * mi;
          const f32x2 up = au * inv + (f32x2){cu[g][2 * r], cu[g][2 * r + 1]} * mi;
          gt[0] = gt[0] > 0.f ? gt[0] : 0.f;
          gt[1] = gt[1] > 0.f ? gt[1] : 0.f;
          o[2 * g + r] = pk2(gt * up);
        }
      }
      auto s0 = __builtin_amdgcn_permlane32_swap(o[0], o[2], false, false);
      auto s1 = __builtin_amdgcn_permlane32_swap(o[1], o[3], false, false);
      u32x4 ov = {s0[0], s1[0], s0[1], s1[1]};
      *(u32x4*)(orow + (size_t)mb * 32 * F_) = ov;
    }
  }
  STAMP(2,6);
  DRAIN();
  STAMP(2,7);
}

static constexpr int K4_STAGE = 32768;

__global__ __launch_bounds__(384) void k4_gemm2(const u16* __restrict__ hh, const u16* __restrict__ wd,
                                                const u16* __restrict__ hT, u16* __restrict__ out) {
  __shared__ __attribute__((aligned(16))) unsigned char smem_[4 * K4_STAGE];
  lds_u8* smem = (lds_u8*)smem_;
  const int t = threadIdx.x, lane = t & 63;
  const int w = __builtin_amdgcn_readfirstlane(t >> 6);
  const int b = blockIdx.x, x = b & 7, idx = b >> 3;
  const int mt = x * 4 + (idx & 3), nt = idx >> 2;
  const int m0 = mt * 64, n0 = nt * 64;
  STAMP(3,0);
  f32x16 acc[2][2];
  u32x4 hr0, hr1;
  const int hf = lane >> 5, r32 = lane & 31;
  if (w >= 4) {
    const unsigned sbase = (unsigned)(size_t)smem + (w - 4) * 16384;
    unsigned voff[4];
    const char* base;
    size_t pstride, sstride;
    if (w == 4) {
      const int ar = lane >> 4, as = lane & 15;
#pragma unroll
      for (int i = 0; i < 4; ++i) voff[i] = ((4 * i + ar) * F_ + (as ^ (4 * i + ar)) * 8) * 2;
      base = (const char*)(hh + (size_t)m0 * F_);
      pstride = (size_t)16 * F_ * 2; sstride = 256;
    } else {
      const int br = lane >> 3, bs = lane & 7;
#pragma unroll
      for (int i = 0; i < 4; ++i) voff[i] = ((8 * i + br) * D_ + (bs ^ (((br >> 1) & 1) << 2)) * 8) * 2;
      base = (const char*)(wd + n0);
      pstride = (size_t)32 * D_ * 2; sstride = (size_t)128 * D_ * 2;
    }
    auto issue = [&](int st) {
      const unsigned sb = sbase + (st & 3) * K4_STAGE;
      const char* pp = base + st * sstride;
#pragma unroll
      for (int i = 0; i < 16; ++i) glds16(pp + (i >> 2) * pstride, voff[i & 3], sb + i * 1024);
    };
    issue(0); issue(1);
#pragma unroll 1
    for (int st = 0; st < 14; ++st) {
      if (st == 0 || st == 12) WAITV(16); else if (st < 12) WAITV(32); else WAITV(0);
      __builtin_amdgcn_s_barrier();
      if (st == 0) issue(2);
      if (st + 3 < 14) issue(st + 3);
    }
  } else {
    hr0 = *(const u32x4*)(hT + (n0 + lane) * S_ + m0 + 16 * w);
    hr1 = *(const u32x4*)(hT + (n0 + lane) * S_ + m0 + 16 * w + 8);
#pragma unroll
    for (int i = 0; i < 2; ++i)
#pragma unroll
      for (int j = 0; j < 2; ++j)
#pragma unroll
        for (int e = 0; e < 16; ++e) acc[i][j][e] = 0.f;
    const int bq = (lane & 15) >> 2;
    const int bsw = ((bq >> 1) & 1) << 2;
    const int bcol = 16 * ((lane >> 4) & 1) + 4 * (lane & 3);
    const int boff0 = 16384 + (32 * w + 8 * hf + bq) * 128 + (((bcol >> 3) ^ bsw) * 16) + (bcol & 7) * 2;
    const int boff1 = 16384 + (32 * w + 8 * hf + bq) * 128 + ((((bcol + 32) >> 3) ^ bsw) * 16) + (bcol & 7) * 2;
    const int aoff0 = r32 * 256 + (((4 * w + hf) ^ (r32 & 15)) * 16);
    const int aoff1 = r32 * 256 + (((4 * w + 2 + hf) ^ (r32 & 15)) * 16);
#pragma unroll 1
    for (int st = 0; st < 14; ++st) {
      if (st == 0) STAMP(3,1);
      __builtin_amdgcn_s_barrier();
      if (st == 0) STAMP(3,2);
      if (st == 1) STAMP(3,3);
      if (st == 7) STAMP(3,4);
      const lds_u8* sb = smem + (st & 3) * K4_STAGE;
      bf16x8 af[2][2], bf[2][2];
#pragma unroll
      for (int mb = 0; mb < 2; ++mb) {
        af[0][mb] = *(const __attribute__((address_space(3))) bf16x8*)(sb + aoff0 + mb * 8192);
        af[1][mb] = *(const __attribute__((address_space(3))) bf16x8*)(sb + aoff1 + mb * 8192);
      }
#pragma unroll
      for (int ks = 0; ks < 2; ++ks) {
        bf[ks][0] = tr_frag(sb + boff0 + ks * 16 * 128, 4 * 128);
        bf[ks][1] = tr_frag(sb + boff1 + ks * 16 * 128, 4 * 128);
      }
#pragma unroll
      for (int ks = 0; ks < 2; ++ks)
#pragma unroll
        for (int mb = 0; mb < 2; ++mb)
#pragma unroll
          for (int nb = 0; nb < 2; ++nb)
            acc[mb][nb] = __builtin_amdgcn_mfma_f32_32x32x16_bf16(af[ks][mb], bf[ks][nb], acc[mb][nb], 0, 0, 0);
    }
  }
  STAMP(3,5);
  __syncthreads();
  float* red = (float*)smem_;
  if (w < 4) {
#pragma unroll
    for (int mb = 0; mb < 2; ++mb)
#pragma unroll
      for (int nb = 0; nb < 2; ++nb)
#pragma unroll
        for (int g = 0; g < 4; ++g)
          *(f32x4*)(red + w * 4352 + (32 * nb + r32) * 68 + 32 * mb + 8 * g + 4 * hf) =
              (f32x4){acc[mb][nb][4 * g], acc[mb][nb][4 * g + 1], acc[mb][nb][4 * g + 2], acc[mb][nb][4 * g + 3]};
  }
  __syncthreads();
  u16* ob = (u16*)(smem_ + 71680);
  if (w < 4) {
#pragma unroll
    for (int i = 0; i < 4; ++i) {
      const float* rp = red + lane * 68 + 16 * w + 4 * i;
      const f32x4 s = (*(const f32x4*)(rp) + *(const f32x4*)(rp + 4352)) + (*(const f32x4*)(rp + 8704) + *(const f32x4*)(rp + 13056));
      const unsigned h0 = i < 2 ? hr0[2 * i] : hr1[2 * i - 4];
      const unsigned h1 = i < 2 ? hr0[2 * i + 1] : hr1[2 * i - 3];
      const unsigned o0 = pk2(unpk(h0) + rbf2((f32x2){s[0], s[1]}));
      const unsigned o1 = pk2(unpk(h1) + rbf2((f32x2){s[2], s[3]}));
      u16* op = ob + (16 * w + 4 * i) * 72 + lane;
      op[0] = (u16)o0; op[72] = (u16)(o0 >> 16); op[144] = (u16)o1; op[216] = (u16)(o1 >> 16);
    }
  }
  __syncthreads();
  if (w < 4) {
#pragma unroll
    for (int i = 0; i < 2; ++i) {
      const int p = t + 256 * i, row = p >> 3, c = p & 7;
      *(u32x4*)(out + (m0 + row) * D_ + n0 + c * 8) = *(const u32x4*)(ob + row * 72 + c * 8);
    }
  }
  STAMP(3,6);
  DRAIN();
  STAMP(3,7);
}

extern "C" void kernel_launch(void* const* d_in, const int* in_sizes, int n_in, void* d_out, int out_size,
                              void* d_ws, size_t ws_size, hipStream_t stream) {
  const u16* q = (const u16*)d_in[0];
  const u16* k = (const u16*)d_in[1];
  const u16* v = (const u16*)d_in[2];
  const u16* shk = (const u16*)d_in[3];
  const u16* ssmk = (const u16*)d_in[4];
  const u16* ssmD = (const u16*)d_in[5];
  const u16* wg = (const u16*)d_in[7];
  const u16* wu = (const u16*)d_in[8];
  const u16* wd = (const u16*)d_in[9];
  unsigned char* ws = (unsigned char*)d_ws;
  u16* kvR = (u16*)(ws);
  u16* xT = (u16*)(ws + (2u << 20));
  float* qm = (float*)(ws + (4u << 20));
  float* qinv = qm + 2048;
  float* csp = (float*)(ws + (4u << 20) + 65536);
  u16* h = (u16*)(ws + (5u << 20));
  u16* hh = (u16*)(ws + (8u << 20));
  u16* wp = (u16*)(ws + (16u << 20));
  k1_prep<<<572, 512, 0, stream>>>(q, k, v, shk, wg, wu, kvR, xT, qm, qinv, wp, csp, ssmk, wd, (unsigned*)(ws + (24u << 20)));
  k2_conv<<<256, 512, 0, stream>>>(ssmk, ssmD, kvR, xT, qm, qinv, h);
  k3_gemm1<<<256, 512, 0, stream>>>(h, wp, csp, hh);
  k4_gemm2<<<256, 384, 0, stream>>>(hh, wd, h, (u16*)d_out);
}
```

```cpp
#include <hip/hip_runtime.h>
#include <hip/hip_bf16.h>
#include <stdint.h>

typedef unsigned short u16;
typedef __attribute__((ext_vector_type(8))) short bf16x8;
typedef __attribute__((ext_vector_type(4))) short s16x4;
typedef __attribute__((ext_vector_type(16))) float f32x16;
typedef __attribute__((ext_vector_type(4))) float f32x4;
typedef __attribute__((ext_vector_type(2))) float f32x2;
typedef __attribute__((ext_vector_type(4))) unsigned u32x4;
typedef __attribute__((ext_vector_type(2))) unsigned u32x2;
typedef __attribute__((ext_vector_type(2))) __bf16 bf16x2_t;
typedef __attribute__((ext_vector_type(4))) short v4i16_t;
typedef __attribute__((address_space(3))) unsigned char lds_u8;

#define DEV __device__ __forceinline__
#define STAMP(kid,i)
#define DRAIN()

static constexpr int S_ = 2048, D_ = 512, F_ = 1792;

DEV float bflo(unsigned x) { return __uint_as_float(x << 16); }
DEV float bfhi(unsigned x) { return __uint_as_float(x & 0xffff0000u); }
DEV f32x2 unpk(unsigned x) { return (f32x2){bflo(x), bfhi(x)}; }
DEV unsigned pk2(f32x2 v) {
  bf16x2_t r = {(__bf16)v[0], (__bf16)v[1]};
  return __builtin_bit_cast(unsigned, r);
}
DEV f32x2 rbf2(f32x2 v) { return unpk(pk2(v)); }
DEV float rbf(float x) { return (float)(__bf16)x; }

DEV void glds16(const void* sbase, unsigned voff, unsigned lds_dst) {
  asm volatile("s_mov_b32 m0, %2\n\ts_nop 0\n\tglobal_load_lds_dwordx4 %0, %1"
               :: "v"(voff), "s"(sbase), "s"(lds_dst) : "memory");
}
#define WAITV(n) asm volatile("s_waitcnt vmcnt(" #n ")" ::: "memory")

DEV s16x4 tr_read(const lds_u8* p) {
  return __builtin_bit_cast(s16x4, __builtin_amdgcn_ds_read_tr16_b64_v4i16((__attribute__((address_space(3))) v4i16_t*)p));
}
DEV bf16x8 tr_frag(const lds_u8* p, int rowstride4) {
  const s16x4 lo = tr_read(p);
  const s16x4 hi = tr_read(p + rowstride4);
  return (bf16x8){lo[0], lo[1], lo[2], lo[3], hi[0], hi[1], hi[2], hi[3]};
}

__global__ __launch_bounds__(512) void k1_prep(const u16* __restrict__ q, const u16* __restrict__ k,
                                               const u16* __restrict__ v, const u16* __restrict__ shk,
                                               u16* __restrict__ kvR, u16* __restrict__ xT,
                                               float* __restrict__ qm, float* __restrict__ qinv) {
  __shared__ __attribute__((aligned(16))) unsigned char smem[2 * 64 * 66 * 2];
  const int b = blockIdx.x, t = threadIdx.x;
  if (b < 256) {
    u16* kvs = (u16*)smem;
    u16* xs = (u16*)(smem + 64 * 66 * 2);
    const int s0 = (b & 31) * 64, d0 = (b >> 5) * 64;
    const int c = t & 7, r = t >> 3, s = s0 + r;
    u32x4 wv[4], kr[4];
#pragma unroll
    for (int i = 0; i < 4; ++i) wv[i] = *(const u32x4*)(shk + (d0 + 8 * c) * 4 + 8 * i);
#pragma unroll
    for (int j = 0; j < 4; ++j) {
      const int ss = s - 3 + j;
      u32x4 z = {0u, 0u, 0u, 0u};
      kr[j] = (ss >= 0) ? *(const u32x4*)(k + ss * D_ + d0 + 8 * c) : z;
    }
    const u32x4 vv = *(const u32x4*)(v + s * D_ + d0 + 8 * c);
    const u32x4 qq = *(const u32x4*)(q + s * D_ + d0 + 8 * c);
#pragma unroll
    for (int p = 0; p < 4; ++p) {
      const f32x2 w0 = {bflo(wv[p][0]), bflo(wv[p][2])}, w1 = {bfhi(wv[p][0]), bfhi(wv[p][2])};
      const f32x2 w2 = {bflo(wv[p][1]), bflo(wv[p][3])}, w3 = {bfhi(wv[p][1]), bfhi(wv[p][3])};
      f32x2 ks = rbf2(unpk(kr[0][p]) * w3);
      ks = rbf2(ks + rbf2(unpk(kr[1][p]) * w2));
      ks = rbf2(ks + rbf2(unpk(kr[2][p]) * w1));
      ks = rbf2(ks + rbf2(unpk(kr[3][p]) * w0));
      const unsigned kvb = pk2(ks * unpk(vv[p]));
      kvs[(8 * c + 2 * p) * 66 + (63 - r)] = (u16)kvb;
      kvs[(8 * c + 2 * p + 1) * 66 + (63 - r)] = (u16)(kvb >> 16);
      xs[(8 * c + 2 * p) * 66 + r] = (u16)qq[p];
      xs[(8 * c + 2 * p + 1) * 66 + r] = (u16)(qq[p] >> 16);
    }
    __syncthreads();
    {
      const int ch = r;
      const unsigned* a = (const unsigned*)(kvs + ch * 66 + 8 * c);
      const unsigned* bq = (const unsigned*)(xs + ch * 66 + 8 * c);
      u32x4 o1 = {a[0], a[1], a[2], a[3]};
      u32x4 o2 = {bq[0], bq[1], bq[2], bq[3]};
      *(u32x4*)(kvR + (d0 + ch) * S_ + (1984 - s0) + 8 * c) = o1;
      *(u32x4*)(xT + (d0 + ch) * S_ + s0 + 8 * c) = o2;
    }
  } else {
    const int lane = t & 63, w = t >> 6;
#pragma unroll
    for (int i = 0; i < 2; ++i) {
      const int row = (b - 256) * 16 + w * 2 + i;
      const u32x4 x = *(const u32x4*)(q + row * D_ + lane * 8);
      f32x2 f[4];
#pragma unroll
      for (int j = 0; j < 4; ++j) f[j] = unpk(x[j]);
      f32x2 s2 = (f[0] + f[1]) + (f[2] + f[3]);
      float sum = s2[0] + s2[1];
#pragma unroll
      for (int o = 32; o >= 1; o >>= 1) sum += __shfl_xor(sum, o);
      const float mean = sum * (1.0f / D_);
      f32x2 q2 = {0.f, 0.f};
#pragma unroll
      for (int j = 0; j < 4; ++j) { const f32x2 d = f[j] - mean; q2 += d * d; }
      float sq = q2[0] + q2[1];
#pragma unroll
      for (int o = 32; o >= 1; o >>= 1) sq += __shfl_xor(sq, o);
      const float var = rbf(sq * (1.0f / D_));
      if (lane == 0) {
        qm[row] = rbf(mean);
        qinv[row] = 1.0f / rbf(sqrtf(rbf(var + 1e-5f)));
      }
    }
  }
}

static constexpr int K2_COPY = 4160;
static constexpr int K2_KC = 8 * K2_COPY;
static constexpr int K2_XR = 96 * 80;
static constexpr int K2_CH = K2_KC + K2_XR;

__global__ __launch_bounds__(512) void k2_conv(const u16* __restrict__ ssmk, const u16* __restrict__ ssmD,
                                               const u16* __restrict__ kvR, const u16* __restrict__ xT,
                                               const float* __restrict__ qm, const float* __restrict__ qinv,
                                               u16* __restrict__ hT,
                                               const u16* __restrict__ wg, const u16* __restrict__ wu,
                                               u16* __restrict__ wp, float* __restrict__ csp,
                                               const u16* __restrict__ wd, unsigned* __restrict__ sink) {
  __shared__ __attribute__((aligned(16))) unsigned char smem[2 * K2_CH];
  const int t = threadIdx.x, lane = t & 63, w = t >> 6;
  if (blockIdx.x >= 384) {
    const u32x4* src = (const u32x4*)wd + (blockIdx.x - 384) * 4096;
    unsigned a = 0;
#pragma unroll
    for (int i = 0; i < 8; ++i) { const u32x4 x = src[i * 512 + t]; a |= x[0] ^ x[1] ^ x[2] ^ x[3]; }
    if (a == 0x9e3779b9u) sink[t] = a;
    return;
  }
  if (blockIdx.x >= 256) {
    lds_u8* tile = (lds_u8*)smem;
    float* red = (float*)(smem + 28672);
    const int bb = blockIdx.x - 256, ft = bb & 15, kc = bb >> 4;
    const int part = t % 28, rg = t / 28;
    if (rg < 16) {
      f32x2 acc[4] = {{0.f, 0.f}, {0.f, 0.f}, {0.f, 0.f}, {0.f, 0.f}};
      const int mat = part >= 14;
      const u16* src = (mat ? wu : wg) + ft * 112 + (part - 14 * mat) * 8;
#pragma unroll
      for (int i = 0; i < 4; ++i) {
        const int kk = rg * 4 + i;
        const u32x4 x = *(const u32x4*)(src + (kc * 64 + kk) * F_);
        *(u32x4*)(smem + kk * 448 + part * 16) = x;
#pragma unroll
        for (int j = 0; j < 4; ++j) acc[j] += unpk(x[j]);
      }
      *(f32x4*)(red + rg * 224 + part * 8) = (f32x4){acc[0][0], acc[0][1], acc[1][0], acc[1][1]};
      *(f32x4*)(red + rg * 224 + part * 8 + 4) = (f32x4){acc[2][0], acc[2][1], acc[3][0], acc[3][1]};
    }
    __syncthreads();
    const int woff = (8 * (lane >> 5) + ((lane & 15) >> 2)) * 448 + ((lane >> 4) & 1) * 224 + (lane & 3) * 8;
#pragma unroll 1
    for (int f = w; f < 28; f += 8) {
      const int j = f >> 2, ks = f & 3;
      const bf16x8 fr = tr_frag(tile + woff + ks * 16 * 448 + j * 32, 4 * 448);
      *(bf16x8*)(wp + ((((size_t)ft * 7 + j) * 32 + kc * 4 + ks) * 64 + lane) * 8) = fr;
    }
    if (t < 224) {
      float s = 0.f;
#pragma unroll
      for (int i = 0; i < 16; ++i) s += red[i * 224 + t];
      csp[(kc * 16 + ft) * 224 + t] = s;
    }
    return;
  }
  const int d0 = blockIdx.x * 2;
  const u32x4 z4 = {0u, 0u, 0u, 0u};
  const int ch = w >> 2, qq = w & 3;
  const int il = lane & 7, ih = (lane >> 3) & 3, hh = lane >> 5, col = lane & 31;
  const int d = d0 + ch;
  STAMP(1,0);
  const int tv = t & 255;
  const u16* kk = ssmk + d * S_;
  u32x4 cur = *(const u32x4*)(kk + tv * 8);
  u32x4 nxt = (tv < 255) ? *(const u32x4*)(kk + tv * 8 + 8) : z4;
  const u32x4 xv = *(const u32x4*)(kvR + d * S_ + tv * 8);
  const int nb0 = 1024 * (qq >> 1) + 32 * col + 16 * (qq & 1) + 4 * hh;
  u32x2 xq[2];
  f32x4 mm[2], dd[2];
#pragma unroll
  for (int e = 0; e < 2; ++e) {
    xq[e] = *(const u32x2*)(xT + d * S_ + nb0 + 8 * e);
    mm[e] = *(const f32x4*)(qm + nb0 + 8 * e);
    dd[e] = *(const f32x4*)(qinv + nb0 + 8 * e);
  }
  const float Dd = bflo(ssmD[d]);
  unsigned char* base = smem + ch * K2_CH;
  *(u32x4*)(base + K2_KC + (tv >> 2) * 80 + (tv & 3) * 16) = xv;
  if (tv < 128) *(u32x4*)(base + K2_KC + (64 + (tv >> 2)) * 80 + (tv & 3) * 16) = z4;
  int unit = tv + 4;
#pragma unroll 1
  for (int rep = 0; rep < 2; ++rep) {
    const unsigned wd[8] = {cur[0], cur[1], cur[2], cur[3], nxt[0], nxt[1], nxt[2], nxt[3]};
#pragma unroll
    for (int sh = 1; sh <= 8; ++sh) {
      u32x4 o;
      const int a = sh >> 1;
#pragma unroll
      for (int i = 0; i < 4; ++i) o[i] = (sh & 1) ? __builtin_amdgcn_alignbit(wd[a + i + 1], wd[a + i], 16) : wd[a + i];
      *(u32x4*)(base + (sh - 1) * K2_COPY + unit * 16) = o;
    }
    if (tv >= 4) break;
    nxt = (tv == 0) ? cur : z4;
    cur = z4;
    unit = (tv + 3) & 3;
  }
  STAMP(1,1);
  __syncthreads();
  STAMP(1,2);
  const unsigned char* kcb = base + il * K2_COPY + (2 + ih + hh + 32 * qq) * 16;
  const unsigned char* xb = base + K2_KC + (63 - col + 8 * qq) * 80 + (16 + 8 * hh) * 2;
  f32x16 acc0, acc1;
#pragma unroll
  for (int i = 0; i < 16; ++i) { acc0[i] = 0.f; acc1[i] = 0.f; }
#pragma unroll 2
  for (int c = 0; c < 8; ++c) {
#pragma unroll
    for (int s = 0; s < 2; ++s) {
      const bf16x8 a0 = *(const bf16x8*)(kcb + (4 * c - 2 * s) * 16);
      const bf16x8 a1 = *(const bf16x8*)(kcb + (4 * c - 2 * s + 128) * 16);
      const bf16x8 b0 = *(const bf16x8*)(xb + c * 80 - 32 * s);
      const bf16x8 b1 = *(const bf16x8*)(xb + (c - 32) * 80 - 32 * s);
      acc0 = __builtin_amdgcn_mfma_f32_32x32x16_bf16(a0, b0, acc0, 0, 0, 0);
      acc1 = __builtin_amdgcn_mfma_f32_32x32x16_bf16(a0, b1, acc1, 0, 0, 0);
      acc1 = __builtin_amdgcn_mfma_f32_32x32x16_bf16(a1, b0, acc1, 0, 0, 0);
    }
  }
  STAMP(1,3);
  __syncthreads();
  f32x4* red = (f32x4*)base;
#pragma unroll
  for (int g = 0; g < 4; ++g) {
    red[(qq * 8 + g) * 64 + lane] = (f32x4){acc0[4 * g], acc0[4 * g + 1], acc0[4 * g + 2], acc0[4 * g + 3]};
    red[(qq * 8 + 4 + g) * 64 + lane] = (f32x4){acc1[4 * g], acc1[4 * g + 1], acc1[4 * g + 2], acc1[4 * g + 3]};
  }
  __syncthreads();
  STAMP(1,4);
  unsigned pk[2][2];
#pragma unroll
  for (int e = 0; e < 2; ++e) {
    const f32x4* rp = red + (2 * qq + e) * 64 + lane;
    const f32x4 y4 = (rp[0] + rp[512]) + (rp[1024] + rp[1536]);
    const int t0 = 2044 - (nb0 + 8 * e);
    const u32x2 kvp = *(const u32x2*)(base + K2_KC + (t0 >> 5) * 80 + (t0 & 31) * 2);
#pragma unroll
    for (int r = 0; r < 2; ++r) {
      const f32x2 kvf = {bfhi(kvp[1 - r]), bflo(kvp[1 - r])};
      const f32x2 xf = unpk(xq[e][r]);
      const f32x2 yy = rbf2((f32x2){y4[2 * r], y4[2 * r + 1]});
      const f32x2 y2 = rbf2(yy + rbf2(kvf * Dd));
      const f32x2 qn = rbf2(rbf2(xf - (f32x2){mm[e][2 * r], mm[e][2 * r + 1]}) * (f32x2){dd[e][2 * r], dd[e][2 * r + 1]});
      pk[e][r] = pk2(xf + rbf2(qn * y2));
    }
  }
  auto s0 = __builtin_amdgcn_permlane32_swap(pk[0][0], pk[1][0], false, false);
  auto s1 = __builtin_amdgcn_permlane32_swap(pk[0][1], pk[1][1], false, false);
  const u32x4 ov = {s0[0], s1[0], s0[1], s1[1]};
  STAMP(1,5);
  *(u32x4*)(hT + d * S_ + 1024 * (qq >> 1) + 32 * col + 16 * (qq & 1) + 8 * hh) = ov;
  STAMP(1,6);
  DRAIN();
  STAMP(1,7);
}

DEV void gload16(bf16x8& dst, const void* sbase, unsigned voff) {
  asm volatile("global_load_dwordx4 %0, %1, %2" : "=v"(dst) : "v"(voff), "s"(sbase) : "memory");
}

template <bool STATS>
DEV void k3_main(lds_u8* smem, const u16* wp, int ft, int w, int lane, float* stl, f32x16* acc) {
  const int hf = lane >> 5, q4 = (lane & 15) >> 2;
  {
    const int bsl = 2 * ((lane >> 4) & 1) + ((lane & 3) >> 1);
    int boff[4];
#pragma unroll
    for (int i = 0; i < 4; ++i)
      boff[i] = (8 * hf + q4) * 256 + (((4 * ((i + w) & 3) + bsl) ^ (q4 << 2)) * 16) + (lane & 1) * 8;
    const char* wbase = (const char*)(wp + ((size_t)ft * 7 + w) * 32 * 512);
    const unsigned voffW = lane * 16;
    bf16x8 wf[32];
#pragma unroll
    for (int i = 0; i < 12; ++i) gload16(wf[i], wbase + i * 1024, voffW);
#pragma unroll
    for (int j = 0; j < 4; ++j)
#pragma unroll
      for (int i = 0; i < 16; ++i) acc[j][i] = 0.f;
    float sum0 = 0.f, sum1 = 0.f, sq0 = 0.f, sq1 = 0.f;
    bf16x8 b0[4], b1[4];
    auto ld = [&](int kg, bf16x8* bb) {
#pragma unroll
      for (int i = 0; i < 4; ++i) bb[i] = tr_frag(smem + kg * 4096 + boff[i], 1024);
    };
    auto mm = [&](int kg, const bf16x8* bb) {
#pragma unroll
      for (int i = 0; i < 4; ++i) acc[i] = __builtin_amdgcn_mfma_f32_32x32x16_bf16(wf[kg], bb[i], acc[i], 0, 0, 0);
      if (STATS) {
        const u32x4 bu = __builtin_bit_cast(u32x4, bb[0]);
#pragma unroll
        for (int i = 0; i < 4; ++i) {
          const unsigned xi = bu[i];
          const float lo = bflo(xi), hi = bfhi(xi);
          sum0 += lo; sum1 += hi;
          sq0 = fmaf(lo, lo, sq0); sq1 = fmaf(hi, hi, sq1);
        }
        asm volatile("" : "+v"(sum0), "+v"(sum1), "+v"(sq0), "+v"(sq1));
      }
    };
    STAMP(2,1);
    asm volatile("s_waitcnt vmcnt(8)" : "+v"(wf[0]), "+v"(wf[1]), "+v"(wf[2]), "+v"(wf[3]) :: "memory");
    __builtin_amdgcn_s_barrier();
    STAMP(2,2);
    ld(0, b0);
#pragma unroll
    for (int c = 0; c < 8; ++c) {
      if (c < 5) {
#pragma unroll
        for (int i = 0; i < 4; ++i) gload16(wf[4 * c + 12 + i], wbase + (4 * c + 12 + i) * 1024, voffW);
      }
      ld(4 * c + 1, b1); mm(4 * c, b0);
      ld(4 * c + 2, b0); mm(4 * c + 1, b1);
      ld(4 * c + 3, b1); mm(4 * c + 2, b0);
      if (c < 7) {
        asm volatile("s_waitcnt vmcnt(%4)" : "+v"(wf[4 * c + 4]), "+v"(wf[4 * c + 5]), "+v"(wf[4 * c + 6]), "+v"(wf[4 * c + 7])
                     : "i"(c < 5 ? 8 : (c == 5 ? 4 : 0)) : "memory");
        __builtin_amdgcn_s_barrier();
        if (c == 0) STAMP(2,3);
        if (c == 3) STAMP(2,4);
        ld(4 * c + 4, b0);
      }
      mm(4 * c + 3, b1);
    }
    STAMP(2,5);
    if (STATS) {
      float sum = sum0 + sum1, sq = sq0 + sq1;
      sum += __shfl_xor(sum, 32);
      sq += __shfl_xor(sq, 32);
      const float mean = sum * (1.0f / D_);
      const float var = rbf(fmaxf(sq * (1.0f / D_) - mean * mean, 0.f));
      const float inv_ = 1.0f / rbf(sqrtf(rbf(var + 1e-5f)));
      if (lane < 32) *(f32x2*)(stl + (w * 32 + lane) * 2) = (f32x2){inv_, -rbf(mean) * inv_};
    }
  }
}

__global__ __launch_bounds__(512) void k3_gemm1(const u16* __restrict__ hT, const u16* __restrict__ wp,
                                                const float* __restrict__ csp, u16* __restrict__ hh,
                                                const u16* __restrict__ wd) {
  __shared__ __attribute__((aligned(16))) unsigned char smem_[131072 + 2048 + 7168];
  lds_u8* smem = (lds_u8*)smem_;
  const unsigned sbase = (unsigned)(size_t)smem;
  const int t = threadIdx.x, lane = t & 63;
  const int w = __builtin_amdgcn_readfirstlane(t >> 6);
  const int b = blockIdx.x, x = b & 7, idx = b >> 3;
  const int mt = (x & 1) * 8 + (idx & 7), ft = (x >> 1) * 4 + (idx >> 3);
  const int m0 = mt * 128, f0 = ft * 112;
  STAMP(2,0);
  float* csl = (float*)(smem_ + 131072);
  float* stl = csl + 224;
  const int hf = lane >> 5;
  f32x16 acc[4];
  if (w < 4) {
    k3_main<true>(smem, wp, ft, w, lane, stl, acc);
  } else if (w < 7) {
    k3_main<false>(smem, wp, ft, w, lane, stl, acc);
  } else {
    const unsigned voffA = ((lane >> 4) * S_ + (((lane & 15) ^ ((lane >> 4) << 2)) * 8)) * 2;
    const char* abase = (const char*)(hT + m0);
    auto issue = [&](int c) {
#pragma unroll
      for (int i = 0; i < 16; ++i) glds16(abase + (size_t)(c * 64 + 4 * i) * S_ * 2, voffA, sbase + c * 16384 + i * 1024);
    };
    bf16x8 cpart[8];
    const unsigned voffC = (lane < 56 ? lane : 55) * 16;
    issue(0);
#pragma unroll
    for (int k8 = 0; k8 < 8; ++k8) gload16(cpart[k8], (const char*)(csp + (k8 * 16 + ft) * 224), voffC);
    issue(1);
    const char* pf = (const char*)wd + idx * 57344;
    auto prefetch = [&](int p0, int n) {
      for (int i = 0; i < n; ++i) glds16(pf + (p0 + i) * 1024, lane * 16, sbase + 131072 + 4096);
    };
#pragma unroll 1
    for (int c = 0; c < 8; ++c) {
      if (c == 0) WAITV(16); else if (c < 6) WAITV(32); else if (c == 6) WAITV(30); else WAITV(28);
      __builtin_amdgcn_s_barrier();
      if (c == 0) issue(2);
      if (c < 5) issue(c + 3);
      if (c == 5 || c == 6) prefetch((c - 5) * 14, 14);
    }
    prefetch(28, 28);
    WAITV(0);
    asm volatile("" : "+v"(cpart[0]), "+v"(cpart[1]), "+v"(cpart[2]), "+v"(cpart[3]),
                      "+v"(cpart[4]), "+v"(cpart[5]), "+v"(cpart[6]), "+v"(cpart[7]));
    f32x4 cs4 = __builtin_bit_cast(f32x4, cpart[0]);
#pragma unroll
    for (int k8 = 1; k8 < 8; ++k8) cs4 += __builtin_bit_cast(f32x4, cpart[k8]);
    if (lane < 56) *(f32x4*)(csl + lane * 4) = cs4;
  }
  __syncthreads();
  if (w < 7) {
    f32x4 cg[2], cu[2];
#pragma unroll
    for (int g = 0; g < 2; ++g) {
      cg[g] = *(const f32x4*)(csl + 16 * w + 8 * g + 4 * hf);
      cu[g] = *(const f32x4*)(csl + 112 + 16 * w + 8 * g + 4 * hf);
    }
    u16* orow = hh + (size_t)(m0 + (lane & 31)) * F_ + f0 + 16 * w + 8 * hf;
#pragma unroll
    for (int i = 0; i < 4; ++i) {
      const int mb = (i + w) & 3;
      const f32x2 st2 = *(const f32x2*)(stl + (mb * 32 + (lane & 31)) * 2);
      const float inv = st2[0], mi = st2[1];
      unsigned o[4];
#pragma unroll
      for (int g = 0; g < 2; ++g) {
#pragma unroll
        for (int r = 0; r < 2; ++r) {
          const f32x2 ag = {acc[i][4 * g + 2 * r], acc[i][4 * g + 2 * r + 1]};
          const f32x2 au = {acc[i][8 + 4 * g + 2 * r], acc[i][8 + 4 * g + 2 * r + 1]};
          f32x2 gt = ag * inv + (f32x2){cg[g][2 * r], cg[g][2 * r + 1]} * mi;
          const f32x2 up = au * inv + (f32x2){cu[g][2 * r], cu[g][2 * r + 1]} * mi;
          gt[0] = gt[0] > 0.f ? gt[0] : 0.f;
          gt[1] = gt[1] > 0.f ? gt[1] : 0.f;
          o[2 * g + r] = pk2(gt * up);
        }
      }
      auto s0 = __builtin_amdgcn_permlane32_swap(o[0], o[2], false, false);
      auto s1 = __builtin_amdgcn_permlane32_swap(o[1], o[3], false, false);
      u32x4 ov = {s0[0], s1[0], s0[1], s1[1]};
      *(u32x4*)(orow + (size_t)mb * 32 * F_) = ov;
    }
  }
  STAMP(2,6);
  DRAIN();
  STAMP(2,7);
}

static constexpr int K4_STAGE = 32768;

__global__ __launch_bounds__(384) void k4_gemm2(const u16* __restrict__ hh, const u16* __restrict__ wd,
                                                const u16* __restrict__ hT, u16* __restrict__ out) {
  __shared__ __attribute__((aligned(16))) unsigned char smem_[4 * K4_STAGE];
  lds_u8* smem = (lds_u8*)smem_;
  const int t = threadIdx.x, lane = t & 63;
  const int w = __builtin_amdgcn_readfirstlane(t >> 6);
  const int b = blockIdx.x, x = b & 7, idx = b >> 3;
  const int mt = x * 4 + (idx & 3), nt = idx >> 2;
  const int m0 = mt * 64, n0 = nt * 64;
  STAMP(3,0);
  f32x16 acc[2][2];
  u32x4 hr0, hr1;
  const int hf = lane >> 5, r32 = lane & 31;
  if (w >= 4) {
    const unsigned sbase = (unsigned)(size_t)smem + (w - 4) * 16384;
    unsigned voff[4];
    const char* base;
    size_t pstride, sstride;
    if (w == 4) {
      const int ar = lane >> 4, as = lane & 15;
#pragma unroll
      for (int i = 0; i < 4; ++i) voff[i] = ((4 * i + ar) * F_ + (as ^ (4 * i + ar)) * 8) * 2;
      base = (const char*)(hh + (size_t)m0 * F_);
      pstride = (size_t)16 * F_ * 2; sstride = 256;
    } else {
      const int br = lane >> 3, bs = lane & 7;
#pragma unroll
      for (int i = 0; i < 4; ++i) voff[i] = ((8 * i + br) * D_ + (bs ^ (((br >> 1) & 1) << 2)) * 8) * 2;
      base = (const char*)(wd + n0);
      pstride = (size_t)32 * D_ * 2; sstride = (size_t)128 * D_ * 2;
    }
    auto issue = [&](int st) {
      const unsigned sb = sbase + (st & 3) * K4_STAGE;
      const char* pp = base + st * sstride;
#pragma unroll
      for (int i = 0; i < 16; ++i) glds16(pp + (i >> 2) * pstride, voff[i & 3], sb + i * 1024);
    };
    issue(0); issue(1);
#pragma unroll 1
    for (int st = 0; st < 14; ++st) {
      if (st == 0 || st == 12) WAITV(16); else if (st < 12) WAITV(32); else WAITV(0);
      __builtin_amdgcn_s_barrier();
      if (st == 0) issue(2);
      if (st + 3 < 14) issue(st + 3);
    }
  } else {
    hr0 = *(const u32x4*)(hT + (n0 + lane) * S_ + m0 + 16 * w);
    hr1 = *(const u32x4*)(hT + (n0 + lane) * S_ + m0 + 16 * w + 8);
#pragma unroll
    for (int i = 0; i < 2; ++i)
#pragma unroll
      for (int j = 0; j < 2; ++j)
#pragma unroll
        for (int e = 0; e < 16; ++e) acc[i][j][e] = 0.f;
    const int bq = (lane & 15) >> 2;
    const int bsw = ((bq >> 1) & 1) << 2;
    const int bcol = 16 * ((lane >> 4) & 1) + 4 * (lane & 3);
    const int boff0 = 16384 + (32 * w + 8 * hf + bq) * 128 + (((bcol >> 3) ^ bsw) * 16) + (bcol & 7) * 2;
    const int boff1 = 16384 + (32 * w + 8 * hf + bq) * 128 + ((((bcol + 32) >> 3) ^ bsw) * 16) + (bcol & 7) * 2;
    const int aoff0 = r32 * 256 + (((4 * w + hf) ^ (r32 & 15)) * 16);
    const int aoff1 = r32 * 256 + (((4 * w + 2 + hf) ^ (r32 & 15)) * 16);
#pragma unroll 1
    for (int st = 0; st < 14; ++st) {
      if (st == 0) STAMP(3,1);
      __builtin_amdgcn_s_barrier();
      if (st == 0) STAMP(3,2);
      if (st == 1) STAMP(3,3);
      if (st == 7) STAMP(3,4);
      const lds_u8* sb = smem + (st & 3) * K4_STAGE;
      bf16x8 af[2][2], bf[2][2];
#pragma unroll
      for (int mb = 0; mb < 2; ++mb) {
        af[0][mb] = *(const __attribute__((address_space(3))) bf16x8*)(sb + aoff0 + mb * 8192);
        af[1][mb] = *(const __attribute__((address_space(3))) bf16x8*)(sb + aoff1 + mb * 8192);
      }
#pragma unroll
      for (int ks = 0; ks < 2; ++ks) {
        bf[ks][0] = tr_frag(sb + boff0 + ks * 16 * 128, 4 * 128);
        bf[ks][1] = tr_frag(sb + boff1 + ks * 16 * 128, 4 * 128);
      }
#pragma unroll
      for (int ks = 0; ks < 2; ++ks)
#pragma unroll
        for (int mb = 0; mb < 2; ++mb)
#pragma unroll
          for (int nb = 0; nb < 2; ++nb)
            acc[mb][nb] = __builtin_amdgcn_mfma_f32_32x32x16_bf16(af[ks][mb], bf[ks][nb], acc[mb][nb], 0, 0, 0);
    }
  }
  STAMP(3,5);
  __syncthreads();
  float* red = (float*)smem_;
  if (w < 4) {
#pragma unroll
    for (int mb = 0; mb < 2; ++mb)
#pragma unroll
      for (int nb = 0; nb < 2; ++nb)
#pragma unroll
        for (int g = 0; g < 4; ++g)
          *(f32x4*)(red + w * 4352 + (32 * nb + r32) * 68 + 32 * mb + 8 * g + 4 * hf) =
              (f32x4){acc[mb][nb][4 * g], acc[mb][nb][4 * g + 1], acc[mb][nb][4 * g + 2], acc[mb][nb][4 * g + 3]};
  }
  __syncthreads();
  u16* ob = (u16*)(smem_ + 71680);
  if (w < 4) {
#pragma unroll
    for (int i = 0; i < 4; ++i) {
      const float* rp = red + lane * 68 + 16 * w + 4 * i;
      const f32x4 s = (*(const f32x4*)(rp) + *(const f32x4*)(rp + 4352)) + (*(const f32x4*)(rp + 8704) + *(const f32x4*)(rp + 13056));
      const unsigned h0 = i < 2 ? hr0[2 * i] : hr1[2 * i - 4];
      const unsigned h1 = i < 2 ? hr0[2 * i + 1] : hr1[2 * i - 3];
      const unsigned o0 = pk2(unpk(h0) + rbf2((f32x2){s[0], s[1]}));
      const unsigned o1 = pk2(unpk(h1) + rbf2((f32x2){s[2], s[3]}));
      u16* op = ob + (16 * w + 4 * i) * 72 + lane;
      op[0] = (u16)o0; op[72] = (u16)(o0 >> 16); op[144] = (u16)o1; op[216] = (u16)(o1 >> 16);
    }
  }
  __syncthreads();
  if (w < 4) {
#pragma unroll
    for (int i = 0; i < 2; ++i) {
      const int p = t + 256 * i, row = p >> 3, c = p & 7;
      *(u32x4*)(out + (m0 + row) * D_ + n0 + c * 8) = *(const u32x4*)(ob + row * 72 + c * 8);
    }
  }
  STAMP(3,6);
  DRAIN();
  STAMP(3,7);
}

extern "C" void kernel_launch(void* const* d_in, const int* in_sizes, int n_in, void* d_out, int out_size,
                              void* d_ws, size_t ws_size, hipStream_t stream) {
  const u16* q = (const u16*)d_in[0];
  const u16* k = (const u16*)d_in[1];
  const u16* v = (const u16*)d_in[2];
  const u16* shk = (const u16*)d_in[3];
  const u16* ssmk = (const u16*)d_in[4];
  const u16* ssmD = (const u16*)d_in[5];
  const u16* wg = (const u16*)d_in[7];
  const u16* wu = (const u16*)d_in[8];
  const u16* wd = (const u16*)d_in[9];
  unsigned char* ws = (unsigned char*)d_ws;
  u16* kvR = (u16*)(ws);
  u16* xT = (u16*)(ws + (2u << 20));
  float* qm = (float*)(ws + (4u << 20));
  float* qinv = qm + 2048;
  float* csp = (float*)(ws + (4u << 20) + 65536);
  u16* h = (u16*)(ws + (5u << 20));
  u16* hh = (u16*)(ws + (8u << 20));
  u16* wp = (u16*)(ws + (16u << 20));
  k1_prep<<<384, 512, 0, stream>>>(q, k, v, shk, kvR, xT, qm, qinv);
  k2_conv<<<412, 512, 0, stream>>>(ssmk, ssmD, kvR, xT, qm, qinv, h, wg, wu, wp, csp, wd, (unsigned*)(ws + (24u << 20)));
  k3_gemm1<<<256, 512, 0, stream>>>(h, wp, csp, hh, wd);
  k4_gemm2<<<256, 384, 0, stream>>>(hh, wd, h, (u16*)d_out);
}
```

```cpp
#include <hip/hip_runtime.h>
#include <hip/hip_bf16.h>
#include <stdint.h>

typedef unsigned short u16;
typedef __attribute__((ext_vector_type(8))) short bf16x8;
typedef __attribute__((ext_vector_type(4))) short s16x4;
typedef __attribute__((ext_vector_type(16))) float f32x16;
typedef __attribute__((ext_vector_type(4))) float f32x4;
typedef __attribute__((ext_vector_type(2))) float f32x2;
typedef __attribute__((ext_vector_type(4))) unsigned u32x4;
typedef __attribute__((ext_vector_type(2))) unsigned u32x2;
typedef __attribute__((ext_vector_type(2))) __bf16 bf16x2_t;
typedef __attribute__((ext_vector_type(4))) short v4i16_t;
typedef __attribute__((address_space(3))) unsigned char lds_u8;

#define DEV __device__ __forceinline__
#define STAMP(kid,i)
#define DRAIN()

static constexpr int S_ = 2048, D_ = 512, F_ = 1792;

DEV float bflo(unsigned x) { return __uint_as_float(x << 16); }
DEV float bfhi(unsigned x) { return __uint_as_float(x & 0xffff0000u); }
DEV f32x2 unpk(unsigned x) { return (f32x2){bflo(x), bfhi(x)}; }
DEV unsigned pk2(f32x2 v) {
  bf16x2_t r = {(__bf16)v[0], (__bf16)v[1]};
  return __builtin_bit_cast(unsigned, r);
}
DEV f32x2 rbf2(f32x2 v) { return unpk(pk2(v)); }
DEV float rbf(float x) { return (float)(__bf16)x; }

DEV void glds16(const void* sbase, unsigned voff, unsigned lds_dst) {
  asm volatile("s_mov_b32 m0, %2\n\ts_nop 0\n\tglobal_load_lds_dwordx4 %0, %1"
               :: "v"(voff), "s"(sbase), "s"(lds_dst) : "memory");
}
#define WAITV(n) asm volatile("s_waitcnt vmcnt(" #n ")" ::: "memory")

DEV s16x4 tr_read(const lds_u8* p) {
  return __builtin_bit_cast(s16x4, __builtin_amdgcn_ds_read_tr16_b64_v4i16((__attribute__((address_space(3))) v4i16_t*)p));
}
DEV bf16x8 tr_frag(const lds_u8* p, int rowstride4) {
  const s16x4 lo = tr_read(p);
  const s16x4 hi = tr_read(p + rowstride4);
  return (bf16x8){lo[0], lo[1], lo[2], lo[3], hi[0], hi[1], hi[2], hi[3]};
}

__global__ __launch_bounds__(512) void k1_prep(const u16* __restrict__ q, const u16* __restrict__ k,
                                               const u16* __restrict__ v, const u16* __restrict__ shk,
                                               u16* __restrict__ kvR, u16* __restrict__ xT,
                                               float* __restrict__ qm, float* __restrict__ qinv) {
  __shared__ __attribute__((aligned(16))) unsigned char smem[2 * 64 * 66 * 2];
  const int b = blockIdx.x, t = threadIdx.x;
  if (b < 256) {
    u16* kvs = (u16*)smem;
    u16* xs = (u16*)(smem + 64 * 66 * 2);
    const int s0 = (b & 31) * 64, d0 = (b >> 5) * 64;
    const int c = t & 7, r = t >> 3, s = s0 + r;
    u32x4 wv[4], kr[4];
#pragma unroll
    for (int i = 0; i < 4; ++i) wv[i] = *(const u32x4*)(shk + (d0 + 8 * c) * 4 + 8 * i);
#pragma unroll
    for (int j = 0; j < 4; ++j) {
      const int ss = s - 3 + j;
      u32x4 z = {0u, 0u, 0u, 0u};
      kr[j] = (ss >= 0) ? *(const u32x4*)(k + ss * D_ + d0 + 8 * c) : z;
    }
    const u32x4 vv = *(const u32x4*)(v + s * D_ + d0 + 8 * c);
    const u32x4 qq = *(const u32x4*)(q + s * D_ + d0 + 8 * c);
#pragma unroll
    for (int p = 0; p < 4; ++p) {
      const f32x2 w0 = {bflo(wv[p][0]), bflo(wv[p][2])}, w1 = {bfhi(wv[p][0]), bfhi(wv[p][2])};
      const f32x2 w2 = {bflo(wv[p][1]), bflo(wv[p][3])}, w3 = {bfhi(wv[p][1]), bfhi(wv[p][3])};
      f32x2 ks = rbf2(unpk(kr[0][p]) * w3);
      ks = rbf2(ks + rbf2(unpk(kr[1][p]) * w2));
      ks = rbf2(ks + rbf2(unpk(kr[2][p]) * w1));
      ks = rbf2(ks + rbf2(unpk(kr[3][p]) * w0));
      const unsigned kvb = pk2(ks * unpk(vv[p]));
      kvs[(8 * c + 2 * p) * 66 + (63 - r)] = (u16)kvb;
      kvs[(8 * c + 2 * p + 1) * 66 + (63 - r)] = (u16)(kvb >> 16);
      xs[(8 * c + 2 * p) * 66 + r] = (u16)qq[p];
      xs[(8 * c + 2 * p + 1) * 66 + r] = (u16)(qq[p] >> 16);
    }
    __syncthreads();
    {
      const int ch = r;
      const unsigned* a = (const unsigned*)(kvs + ch * 66 + 8 * c);
      const unsigned* bq = (const unsigned*)(xs + ch * 66 + 8 * c);
      u32x4 o1 = {a[0], a[1], a[2], a[3]};
      u32x4 o2 = {bq[0], bq[1], bq[2], bq[3]};
      *(u32x4*)(kvR + (d0 + ch) * S_ + (1984 - s0) + 8 * c) = o1;
      *(u32x4*)(xT + (d0 + ch) * S_ + s0 + 8 * c) = o2;
    }
  } else {
    const int lane = t & 63, w = t >> 6;
#pragma unroll
    for (int i = 0; i < 2; ++i) {
      const int row = (b - 256) * 16 + w * 2 + i;
      const u32x4 x = *(const u32x4*)(q + row * D_ + lane * 8);
      f32x2 f[4];
#pragma unroll
      for (int j = 0; j < 4; ++j) f[j] = unpk(x[j]);
      f32x2 s2 = (f[0] + f[1]) + (f[2] + f[3]);
      float sum = s2[0] + s2[1];
#pragma unroll
      for (int o = 32; o >= 1; o >>= 1) sum += __shfl_xor(sum, o);
      const float mean = sum * (1.0f / D_);
      f32x2 q2 = {0.f, 0.f};
#pragma unroll
      for (int j = 0; j < 4; ++j) { const f32x2 d = f[j] - mean; q2 += d * d; }
      float sq = q2[0] + q2[1];
#pragma unroll
      for (int o = 32; o >= 1; o >>= 1) sq += __shfl_xor(sq, o);
      const float var = rbf(sq * (1.0f / D_));
      if (lane == 0) {
        qm[row] = rbf(mean);
        qinv[row] = 1.0f / rbf(sqrtf(rbf(var + 1e-5f)));
      }
    }
  }
}

static constexpr int K2_COPY = 4160;
static constexpr int K2_KC = 8 * K2_COPY;
static constexpr int K2_XR = 96 * 80;
static constexpr int K2_CH = K2_KC + K2_XR;

__global__ __launch_bounds__(512) void k2_conv(const u16* __restrict__ ssmk, const u16* __restrict__ ssmD,
                                               const u16* __restrict__ kvR, const u16* __restrict__ xT,
                                               const float* __restrict__ qm, const float* __restrict__ qinv,
                                               u16* __restrict__ hT,
                                               const u16* __restrict__ wg, const u16* __restrict__ wu,
                                               u16* __restrict__ wp, float* __restrict__ csp,
                                               const u16* __restrict__ wd, unsigned* __restrict__ sink) {
  __shared__ __attribute__((aligned(16))) unsigned char smem[2 * K2_CH];
  const int t = threadIdx.x, lane = t & 63, w = t >> 6;
  if (blockIdx.x >= 384) {
    const u32x4* src = (const u32x4*)wd + (blockIdx.x - 384) * 4096;
    unsigned a = 0;
#pragma unroll
    for (int i = 0; i < 8; ++i) { const u32x4 x = src[i * 512 + t]; a |= x[0] ^ x[1] ^ x[2] ^ x[3]; }
    if (a == 0x9e3779b9u) sink[t] = a;
    return;
  }
  if (blockIdx.x >= 256) {
    lds_u8* tile = (lds_u8*)smem;
    float* red = (float*)(smem + 28672);
    const int bb = blockIdx.x - 256, ft = bb & 15, kc = bb >> 4;
    const int part = t % 28, rg = t / 28;
    if (rg < 16) {
      f32x2 acc[4] = {{0.f, 0.f}, {0.f, 0.f}, {0.f, 0.f}, {0.f, 0.f}};
      const int mat = part >= 14;
      const u16* src = (mat ? wu : wg) + ft * 112 + (part - 14 * mat) * 8;
#pragma unroll
      for (int i = 0; i < 4; ++i) {
        const int kk = rg * 4 + i;
        const u32x4 x = *(const u32x4*)(src + (kc * 64 + kk) * F_);
        *(u32x4*)(smem + kk * 448 + part * 16) = x;
#pragma unroll
        for (int j = 0; j < 4; ++j) acc[j] += unpk(x[j]);
      }
      *(f32x4*)(red + rg * 224 + part * 8) = (f32x4){acc[0][0], acc[0][1], acc[1][0], acc[1][1]};
      *(f32x4*)(red + rg * 224 + part * 8 + 4) = (f32x4){acc[2][0], acc[2][1], acc[3][0], acc[3][1]};
    }
    __syncthreads();
    const int woff = (8 * (lane >> 5) + ((lane & 15) >> 2)) * 448 + ((lane >> 4) & 1) * 224 + (lane & 3) * 8;
#pragma unroll 1
    for (int f = w; f < 28; f += 8) {
      const int j = f >> 2, ks = f & 3;
      const bf16x8 fr = tr_frag(tile + woff + ks * 16 * 448 + j * 32, 4 * 448);
      *(bf16x8*)(wp + ((((size_t)ft * 7 + j) * 32 + kc * 4 + ks) * 64 + lane) * 8) = fr;
    }
    if (t < 224) {
      float s = 0.f;
#pragma unroll
      for (int i = 0; i < 16; ++i) s += red[i * 224 + t];
      csp[(kc * 16 + ft) * 224 + t] = s;
    }
    return;
  }
  const int d0 = blockIdx.x * 2;
  const u32x4 z4 = {0u, 0u, 0u, 0u};
  const int ch = w >> 2, qq = w & 3;
  const int il = lane & 7, ih = (lane >> 3) & 3, hh = lane >> 5, col = lane & 31;
  const int d = d0 + ch;
  STAMP(1,0);
  const int tv = t & 255;
  const u16* kk = ssmk + d * S_;
  u32x4 cur = *(const u32x4*)(kk + tv * 8);
  u32x4 nxt = (tv < 255) ? *(const u32x4*)(kk + tv * 8 + 8) : z4;
  const u32x4 xv = *(const u32x4*)(kvR + d * S_ + tv * 8);
  const int nb0 = 1024 * (qq >> 1) + 32 * col + 16 * (qq & 1) + 4 * hh;
  u32x2 xq[2];
  f32x4 mm[2], dd[2];
#pragma unroll
  for (int e = 0; e < 2; ++e) {
    xq[e] = *(const u32x2*)(xT + d * S_ + nb0 + 8 * e);
    mm[e] = *(const f32x4*)(qm + nb0 + 8 * e);
    dd[e] = *(const f32x4*)(qinv + nb0 + 8 * e);
  }
  const float Dd = bflo(ssmD[d]);
  unsigned char* base = smem + ch * K2_CH;
  *(u32x4*)(base + K2_KC + (tv >> 2) * 80 + (tv & 3) * 16) = xv;
  if (tv < 128) *(u32x4*)(base + K2_KC + (64 + (tv >> 2)) * 80 + (tv & 3) * 16) = z4;
  int unit = tv + 4;
#pragma unroll 1
  for (int rep = 0; rep < 2; ++rep) {
    const unsigned wd[8] = {cur[0], cur[1], cur[2], cur[3], nxt[0], nxt[1], nxt[2], nxt[3]};
#pragma unroll
    for (int sh = 1; sh <= 8; ++sh) {
      u32x4 o;
      const int a = sh >> 1;
#pragma unroll
      for (int i = 0; i < 4; ++i) o[i] = (sh & 1) ? __builtin_amdgcn_alignbit(wd[a + i + 1], wd[a + i], 16) : wd[a + i];
      *(u32x4*)(base + (sh - 1) * K2_COPY + unit * 16) = o;
    }
    if (tv >= 4) break;
    nxt = (tv == 0) ? cur : z4;
    cur = z4;
    unit = (tv + 3) & 3;
  }
  STAMP(1,1);
  __syncthreads();
  STAMP(1,2);
  const unsigned char* kcb = base + il * K2_COPY + (2 + ih + hh + 32 * qq) * 16;
  const unsigned char* xb = base + K2_KC + (63 - col + 8 * qq) * 80 + (16 + 8 * hh) * 2;
  f32x16 acc0, acc1;
#pragma unroll
  for (int i = 0; i < 16; ++i) { acc0[i] = 0.f; acc1[i] = 0.f; }
#pragma unroll 2
  for (int c = 0; c < 8; ++c) {
#pragma unroll
    for (int s = 0; s < 2; ++s) {
      const bf16x8 a0 = *(const bf16x8*)(kcb + (4 * c - 2 * s) * 16);
      const bf16x8 a1 = *(const bf16x8*)(kcb + (4 * c - 2 * s + 128) * 16);
      const bf16x8 b0 = *(const bf16x8*)(xb + c * 80 - 32 * s);
      const bf16x8 b1 = *(const bf16x8*)(xb + (c - 32) * 80 - 32 * s);
      acc0 = __builtin_amdgcn_mfma_f32_32x32x16_bf16(a0, b0, acc0, 0, 0, 0);
      acc1 = __builtin_amdgcn_mfma_f32_32x32x16_bf16(a0, b1, acc1, 0, 0, 0);
      acc1 = __builtin_amdgcn_mfma_f32_32x32x16_bf16(a1, b0, acc1, 0, 0, 0);
    }
  }
  STAMP(1,3);
  __syncthreads();
  f32x4* red = (f32x4*)base;
#pragma unroll
  for (int g = 0; g < 4; ++g) {
    red[(qq * 8 + g) * 64 + lane] = (f32x4){acc0[4 * g], acc0[4 * g + 1], acc0[4 * g + 2], acc0[4 * g + 3]};
    red[(qq * 8 + 4 + g) * 64 + lane] = (f32x4){acc1[4 * g], acc1[4 * g + 1], acc1[4 * g + 2], acc1[4 * g + 3]};
  }
  __syncthreads();
  STAMP(1,4);
  unsigned pk[2][2];
#pragma unroll
  for (int e = 0; e < 2; ++e) {
    const f32x4* rp = red + (2 * qq + e) * 64 + lane;
    const f32x4 y4 = (rp[0] + rp[512]) + (rp[1024] + rp[1536]);
    const int t0 = 2044 - (nb0 + 8 * e);
    const u32x2 kvp = *(const u32x2*)(base + K2_KC + (t0 >> 5) * 80 + (t0 & 31) * 2);
#pragma unroll
    for (int r = 0; r < 2; ++r) {
      const f32x2 kvf = {bfhi(kvp[1 - r]), bflo(kvp[1 - r])};
      const f32x2 xf = unpk(xq[e][r]);
      const f32x2 yy = rbf2((f32x2){y4[2 * r], y4[2 * r + 1]});
      const f32x2 y2 = rbf2(yy + rbf2(kvf * Dd));
      const f32x2 qn = rbf2(rbf2(xf - (f32x2){mm[e][2 * r], mm[e][2 * r + 1]}) * (f32x2){dd[e][2 * r], dd[e][2 * r + 1]});
      pk[e][r] = pk2(xf + rbf2(qn * y2));
    }
  }
  auto s0 = __builtin_amdgcn_permlane32_swap(pk[0][0], pk[1][0], false, false);
  auto s1 = __builtin_amdgcn_permlane32_swap(pk[0][1], pk[1][1], false, false);
  const u32x4 ov = {s0[0], s1[0], s0[1], s1[1]};
  STAMP(1,5);
  *(u32x4*)(hT + d * S_ + 1024 * (qq >> 1) + 32 * col + 16 * (qq & 1) + 8 * hh) = ov;
  STAMP(1,6);
  DRAIN();
  STAMP(1,7);
}

DEV void gload16(bf16x8& dst, const void* sbase, unsigned voff) {
  asm volatile("global_load_dwordx4 %0, %1, %2" : "=v"(dst) : "v"(voff), "s"(sbase) : "memory");
}

template <bool STATS>
DEV void k3_main(lds_u8* smem, const u16* wp, int ft, int w, int lane, float* stl, f32x16* acc) {
  const int hf = lane >> 5, q4 = (lane & 15) >> 2;
  {
    const int bsl = 2 * ((lane >> 4) & 1) + ((lane & 3) >> 1);
    int boff[4];
#pragma unroll
    for (int i = 0; i < 4; ++i)
      boff[i] = (8 * hf + q4) * 256 + (((4 * ((i + w) & 3) + bsl) ^ (q4 << 2)) * 16) + (lane & 1) * 8;
    const char* wbase = (const char*)(wp + ((size_t)ft * 7 + w) * 32 * 512);
    const unsigned voffW = lane * 16;
    bf16x8 wf[32];
#pragma unroll
    for (int i = 0; i < 12; ++i) gload16(wf[i], wbase + i * 1024, voffW);
#pragma unroll
    for (int j = 0; j < 4; ++j)
#pragma unroll
      for (int i = 0; i < 16; ++i) acc[j][i] = 0.f;
    float sum0 = 0.f, sum1 = 0.f, sq0 = 0.f, sq1 = 0.f;
    bf16x8 b0[4], b1[4];
    auto ld = [&](int kg, bf16x8* bb) {
#pragma unroll
      for (int i = 0; i < 4; ++i) bb[i] = tr_frag(smem + kg * 4096 + boff[i], 1024);
    };
    auto mm = [&](int kg, const bf16x8* bb) {
#pragma unroll
      for (int i = 0; i < 4; ++i) acc[i] = __builtin_amdgcn_mfma_f32_32x32x16_bf16(wf[kg], bb[i], acc[i], 0, 0, 0);
      if (STATS) {
        const u32x4 bu = __builtin_bit_cast(u32x4, bb[0]);
#pragma unroll
        for (int i = 0; i < 4; ++i) {
          const unsigned xi = bu[i];
          const float lo = bflo(xi), hi = bfhi(xi);
          sum0 += lo; sum1 += hi;
          sq0 = fmaf(lo, lo, sq0); sq1 = fmaf(hi, hi, sq1);
        }
        asm volatile("" : "+v"(sum0), "+v"(sum1), "+v"(sq0), "+v"(sq1));
      }
    };
    STAMP(2,1);
    asm volatile("s_waitcnt vmcnt(8)" : "+v"(wf[0]), "+v"(wf[1]), "+v"(wf[2]), "+v"(wf[3]) :: "memory");
    __builtin_amdgcn_s_barrier();
    STAMP(2,2);
    ld(0, b0);
#pragma unroll
    for (int c = 0; c < 8; ++c) {
      if (c < 5) {
#pragma unroll
        for (int i = 0; i < 4; ++i) gload16(wf[4 * c + 12 + i], wbase + (4 * c + 12 + i) * 1024, voffW);
      }
      ld(4 * c + 1, b1); mm(4 * c, b0);
      ld(4 * c + 2, b0); mm(4 * c + 1, b1);
      ld(4 * c + 3, b1); mm(4 * c + 2, b0);
      if (c < 7) {
        asm volatile("s_waitcnt vmcnt(%4)" : "+v"(wf[4 * c + 4]), "+v"(wf[4 * c + 5]), "+v"(wf[4 * c + 6]), "+v"(wf[4 * c + 7])
                     : "i"(c < 5 ? 8 : (c == 5 ? 4 : 0)) : "memory");
        __builtin_amdgcn_s_barrier();
        if (c == 0) STAMP(2,3);
        if (c == 3) STAMP(2,4);
        ld(4 * c + 4, b0);
      }
      mm(4 * c + 3, b1);
    }
    STAMP(2,5);
    if (STATS) {
      float sum = sum0 + sum1, sq = sq0 + sq1;
      sum += __shfl_xor(sum, 32);
      sq += __shfl_xor(sq, 32);
      const float mean = sum * (1.0f / D_);
      const float var = rbf(fmaxf(sq * (1.0f / D_) - mean * mean, 0.f));
      const float inv_ = 1.0f / rbf(sqrtf(rbf(var + 1e-5f)));
      if (lane < 32) *(f32x2*)(stl + (w * 32 + lane) * 2) = (f32x2){inv_, -rbf(mean) * inv_};
    }
  }
}

__global__ __launch_bounds__(512) void k3_gemm1(const u16* __restrict__ hT, const u16* __restrict__ wp,
                                                const float* __restrict__ csp, u16* __restrict__ hh,
                                                const u16* __restrict__ wd) {
  __shared__ __attribute__((aligned(16))) unsigned char smem_[131072 + 2048 + 7168];
  lds_u8* smem = (lds_u8*)smem_;
  const unsigned sbase = (unsigned)(size_t)smem;
  const int t = threadIdx.x, lane = t & 63;
  const int w = __builtin_amdgcn_readfirstlane(t >> 6);
  const int b = blockIdx.x, x = b & 7, idx = b >> 3;
  const int mt = (x & 1) * 8 + (idx & 7), ft = (x >> 1) * 4 + (idx >> 3);
  const int m0 = mt * 128, f0 = ft * 112;
  STAMP(2,0);
  float* csl = (float*)(smem_ + 131072);
  float* stl = csl + 224;
  const int hf = lane >> 5;
  f32x16 acc[4];
  if (w < 4) {
    k3_main<true>(smem, wp, ft, w, lane, stl, acc);
  } else if (w < 7) {
    k3_main<false>(smem, wp, ft, w, lane, stl, acc);
  } else {
    const unsigned voffA = ((lane >> 4) * S_ + (((lane & 15) ^ ((lane >> 4) << 2)) * 8)) * 2;
    const char* abase = (const char*)(hT + m0);
    auto issue = [&](int c) {
#pragma unroll
      for (int i = 0; i < 16; ++i) glds16(abase + (size_t)(c * 64 + 4 * i) * S_ * 2, voffA, sbase + c * 16384 + i * 1024);
    };
    bf16x8 cpart[8];
    const unsigned voffC = (lane < 56 ? lane : 55) * 16;
    issue(0);
#pragma unroll
    for (int k8 = 0; k8 < 8; ++k8) gload16(cpart[k8], (const char*)(csp + (k8 * 16 + ft) * 224), voffC);
    issue(1);
    const char* pf = (const char*)wd + idx * 57344;
    auto prefetch = [&](int p0, int n) {
      for (int i = 0; i < n; ++i) glds16(pf + (p0 + i) * 1024, lane * 16, sbase + 131072 + 4096);
    };
#pragma unroll 1
    for (int c = 0; c < 8; ++c) {
      if (c == 0) WAITV(16); else if (c < 6) WAITV(32); else if (c == 6) WAITV(30); else WAITV(28);
      __builtin_amdgcn_s_barrier();
      if (c == 0) issue(2);
      if (c < 5) issue(c + 3);
      if (c == 5 || c == 6) prefetch((c - 5) * 14, 14);
    }
    prefetch(28, 28);
    WAITV(0);
    asm volatile("" : "+v"(cpart[0]), "+v"(cpart[1]), "+v"(cpart[2]), "+v"(cpart[3]),
                      "+v"(cpart[4]), "+v"(cpart[5]), "+v"(cpart[6]), "+v"(cpart[7]));
    f32x4 cs4 = __builtin_bit_cast(f32x4, cpart[0]);
#pragma unroll
    for (int k8 = 1; k8 < 8; ++k8) cs4 += __builtin_bit_cast(f32x4, cpart[k8]);
    if (lane < 56) *(f32x4*)(csl + lane * 4) = cs4;
  }
  __syncthreads();
  if (w < 7) {
    f32x4 cg[2], cu[2];
#pragma unroll
    for (int g = 0; g < 2; ++g) {
      cg[g] = *(const f32x4*)(csl + 16 * w + 8 * g + 4 * hf);
      cu[g] = *(const f32x4*)(csl + 112 + 16 * w + 8 * g + 4 * hf);
    }
    u16* orow = hh + (size_t)(m0 + (lane & 31)) * F_ + f0 + 16 * w + 8 * hf;
#pragma unroll
    for (int i = 0; i < 4; ++i) {
      const int mb = (i + w) & 3;
      const f32x2 st2 = *(const f32x2*)(stl + (mb * 32 + (lane & 31)) * 2);
      const float inv = st2[0], mi = st2[1];
      unsigned o[4];
#pragma unroll
      for (int g = 0; g < 2; ++g) {
#pragma unroll
        for (int r = 0; r < 2; ++r) {
          const f32x2 ag = {acc[i][4 * g + 2 * r], acc[i][4 * g + 2 * r + 1]};
          const f32x2 au = {acc[i][8 + 4 * g + 2 * r], acc[i][8 + 4 * g + 2 * r + 1]};
          f32x2 gt = ag * inv + (f32x2){cg[g][2 * r], cg[g][2 * r + 1]} * mi;
          const f32x2 up = au * inv + (f32x2){cu[g][2 * r], cu[g][2 * r + 1]} * mi;
          gt[0] = gt[0] > 0.f ? gt[0] : 0.f;
          gt[1] = gt[1] > 0.f ? gt[1] : 0.f;
          o[2 * g + r] = pk2(gt * up);
        }
      }
      auto s0 = __builtin_amdgcn_permlane32_swap(o[0], o[2], false, false);
      auto s1 = __builtin_amdgcn_permlane32_swap(o[1], o[3], false, false);
      u32x4 ov = {s0[0], s1[0], s0[1], s1[1]};
      *(u32x4*)(orow + (size_t)mb * 32 * F_) = ov;
    }
  }
  STAMP(2,6);
  DRAIN();
  STAMP(2,7);
}

static constexpr int K4_STAGE = 32768;

__global__ __launch_bounds__(384) void k4_gemm2(const u16* __restrict__ hh, const u16* __restrict__ wd,
                                                const u16* __restrict__ hT, u16* __restrict__ out) {
  __shared__ __attribute__((aligned(16))) unsigned char smem_[4 * K4_STAGE];
  lds_u8* smem = (lds_u8*)smem_;
  const int t = threadIdx.x, lane = t & 63;
  const int w = __builtin_amdgcn_readfirstlane(t >> 6);
  const int b = blockIdx.x, x = b & 7, idx = b >> 3;
  const int mt = x * 4 + (idx & 3), nt = idx >> 2;
  const int m0 = mt * 64, n0 = nt * 64;
  STAMP(3,0);
  const int hf = lane >> 5, r32 = lane & 31;
  if (w >= 4) {
    const unsigned sbase = (unsigned)(size_t)smem + (w - 4) * 16384;
    unsigned voff[4];
    const char* base;
    size_t pstride, sstride;
    if (w == 4) {
      const int ar = lane >> 4, as = lane & 15;
#pragma unroll
      for (int i = 0; i < 4; ++i) voff[i] = ((4 * i + ar) * F_ + (as ^ (4 * i + ar)) * 8) * 2;
      base = (const char*)(hh + (size_t)m0 * F_);
      pstride = (size_t)16 * F_ * 2; sstride = 256;
    } else {
      const int br = lane >> 3, bs = lane & 7;
#pragma unroll
      for (int i = 0; i < 4; ++i) voff[i] = ((8 * i + br) * D_ + (bs ^ (((br >> 1) & 1) << 2)) * 8) * 2;
      base = (const char*)(wd + n0);
      pstride = (size_t)32 * D_ * 2; sstride = (size_t)128 * D_ * 2;
    }
    auto issue = [&](int st) {
      const unsigned sb = sbase + (st & 3) * K4_STAGE;
      const char* pp = base + st * sstride;
#pragma unroll
      for (int i = 0; i < 16; ++i) glds16(pp + (i >> 2) * pstride, voff[i & 3], sb + i * 1024);
    };
    issue(0); issue(1);
#pragma unroll 1
    for (int st = 0; st < 14; ++st) {
      if (st == 0 || st == 12) WAITV(16); else if (st < 12) WAITV(32); else WAITV(0);
      __builtin_amdgcn_s_barrier();
      if (st == 0) issue(2);
      if (st + 3 < 14) issue(st + 3);
    }
  } else {
    const int mb = w >> 1, nb = w & 1;
    u32x2 hr[4];
#pragma unroll
    for (int g = 0; g < 4; ++g) hr[g] = *(const u32x2*)(hT + (n0 + 32 * nb + r32) * S_ + m0 + 32 * mb + 8 * g + 4 * hf);
    f32x16 acc;
#pragma unroll
    for (int e = 0; e < 16; ++e) acc[e] = 0.f;
    const int bq = (lane & 15) >> 2;
    const int bsw = ((bq >> 1) & 1) << 2;
    const int bcol = 32 * nb + 16 * ((lane >> 4) & 1) + 4 * (lane & 3);
    const int boff = 16384 + (8 * hf + bq) * 128 + (((bcol >> 3) ^ bsw) * 16) + (bcol & 7) * 2;
    const int arow = (32 * mb + r32) * 256;
    const int asw = r32 & 15;
#pragma unroll 1
    for (int st = 0; st < 14; ++st) {
      if (st == 0) STAMP(3,1);
      __builtin_amdgcn_s_barrier();
      if (st == 0) STAMP(3,2);
      if (st == 1) STAMP(3,3);
      if (st == 7) STAMP(3,4);
      const lds_u8* sb = smem + (st & 3) * K4_STAGE;
      bf16x8 af[8], bf[8];
#pragma unroll
      for (int ks = 0; ks < 8; ++ks) {
        af[ks] = *(const __attribute__((address_space(3))) bf16x8*)(sb + arow + (((2 * ks + hf) ^ asw) * 16));
        bf[ks] = tr_frag(sb + boff + ks * 16 * 128, 4 * 128);
      }
#pragma unroll
      for (int ks = 0; ks < 8; ++ks) acc = __builtin_amdgcn_mfma_f32_32x32x16_bf16(af[ks], bf[ks], acc, 0, 0, 0);
    }
    STAMP(3,5);
    u16* ob = (u16*)(smem_ + 2 * K4_STAGE + w * 4096);
#pragma unroll
    for (int g = 0; g < 4; ++g) {
      const unsigned o0 = pk2(unpk(hr[g][0]) + rbf2((f32x2){acc[4 * g], acc[4 * g + 1]}));
      const unsigned o1 = pk2(unpk(hr[g][1]) + rbf2((f32x2){acc[4 * g + 2], acc[4 * g + 3]}));
      u16* op = ob + (8 * g + 4 * hf) * 40 + r32;
      op[0] = (u16)o0; op[40] = (u16)(o0 >> 16); op[80] = (u16)o1; op[120] = (u16)(o1 >> 16);
    }
    __builtin_amdgcn_wave_barrier();
    asm volatile("s_waitcnt lgkmcnt(0)" ::: "memory");
    const int row = lane >> 1, hc = (lane & 1) * 16;
    const u32x4 v0 = *(const u32x4*)(ob + row * 40 + hc);
    const u32x4 v1 = *(const u32x4*)(ob + row * 40 + hc + 8);
    u16* dst = out + (m0 + 32 * mb + row) * D_ + n0 + 32 * nb + hc;
    *(u32x4*)(dst) = v0;
    *(u32x4*)(dst + 8) = v1;
    STAMP(3,6);
    DRAIN();
    STAMP(3,7);
  }
}

extern "C" void kernel_launch(void* const* d_in, const int* in_sizes, int n_in, void* d_out, int out_size,
                              void* d_ws, size_t ws_size, hipStream_t stream) {
  const u16* q = (const u16*)d_in[0];
  const u16* k = (const u16*)d_in[1];
  const u16* v = (const u16*)d_in[2];
  const u16* shk = (const u16*)d_in[3];
  const u16* ssmk = (const u16*)d_in[4];
  const u16* ssmD = (const u16*)d_in[5];
  const u16* wg = (const u16*)d_in[7];
  const u16* wu = (const u16*)d_in[8];
  const u16* wd = (const u16*)d_in[9];
  unsigned char* ws = (unsigned char*)d_ws;
  u16* kvR = (u16*)(ws);
  u16* xT = (u16*)(ws + (2u << 20));
  float* qm = (float*)(ws + (4u << 20));
  float* qinv = qm + 2048;
  float* csp = (float*)(ws + (4u << 20) + 65536);
  u16* h = (u16*)(ws + (5u << 20));
  u16* hh = (u16*)(ws + (8u << 20));
  u16* wp = (u16*)(ws + (16u << 20));
  k1_prep<<<384, 512, 0, stream>>>(q, k, v, shk, kvR, xT, qm, qinv);
  k2_conv<<<412, 512, 0, stream>>>(ssmk, ssmD, kvR, xT, qm, qinv, h, wg, wu, wp, csp, wd, (unsigned*)(ws + (24u << 20)));
  k3_gemm1<<<256, 512, 0, stream>>>(h, wp, csp, hh, wd);
  k4_gemm2<<<256, 384, 0, stream>>>(hh, wd, h, (u16*)d_out);
}
```

```cpp
#include <hip/hip_runtime.h>
#include <hip/hip_bf16.h>
#include <stdint.h>

typedef unsigned short u16;
typedef __attribute__((ext_vector_type(8))) short bf16x8;
typedef __attribute__((ext_vector_type(4))) short s16x4;
typedef __attribute__((ext_vector_type(16))) float f32x16;
typedef __attribute__((ext_vector_type(4))) float f32x4;
typedef __attribute__((ext_vector_type(2))) float f32x2;
typedef __attribute__((ext_vector_type(4))) unsigned u32x4;
typedef __attribute__((ext_vector_type(2))) unsigned u32x2;
typedef __attribute__((ext_vector_type(2))) __bf16 bf16x2_t;
typedef __attribute__((ext_vector_type(4))) short v4i16_t;
typedef __attribute__((address_space(3))) unsigned char lds_u8;

#define DEV __device__ __forceinline__
#define STAMP(kid,i)
#define DRAIN()

static constexpr int S_ = 2048, D_ = 512, F_ = 1792;

DEV float bflo(unsigned x) { return __uint_as_float(x << 16); }
DEV float bfhi(unsigned x) { return __uint_as_float(x & 0xffff0000u); }
DEV f32x2 unpk(unsigned x) { return (f32x2){bflo(x), bfhi(x)}; }
DEV unsigned pk2(f32x2 v) {
  bf16x2_t r = {(__bf16)v[0], (__bf16)v[1]};
  return __builtin_bit_cast(unsigned, r);
}
DEV f32x2 rbf2(f32x2 v) { return unpk(pk2(v)); }
DEV float rbf(float x) { return (float)(__bf16)x; }

DEV void glds16(const void* sbase, unsigned voff, unsigned lds_dst) {
  asm volatile("s_mov_b32 m0, %2\n\ts_nop 0\n\tglobal_load_lds_dwordx4 %0, %1"
               :: "v"(voff), "s"(sbase), "s"(lds_dst) : "memory");
}
#define WAITV(n) asm volatile("s_waitcnt vmcnt(" #n ")" ::: "memory")

DEV s16x4 tr_read(const lds_u8* p) {
  return __builtin_bit_cast(s16x4, __builtin_amdgcn_ds_read_tr16_b64_v4i16((__attribute__((address_space(3))) v4i16_t*)p));
}
DEV bf16x8 tr_frag(const lds_u8* p, int rowstride4) {
  const s16x4 lo = tr_read(p);
  const s16x4 hi = tr_read(p + rowstride4);
  return (bf16x8){lo[0], lo[1], lo[2], lo[3], hi[0], hi[1], hi[2], hi[3]};
}

__global__ __launch_bounds__(512) void k1_prep(const u16* __restrict__ q, const u16* __restrict__ k,
                                               const u16* __restrict__ v, const u16* __restrict__ shk,
                                               u16* __restrict__ kvR, u16* __restrict__ xT,
                                               float* __restrict__ qm, float* __restrict__ qinv) {
  __shared__ __attribute__((aligned(16))) unsigned char smem[2 * 64 * 66 * 2];
  const int b = blockIdx.x, t = threadIdx.x;
  if (b < 256) {
    u16* kvs = (u16*)smem;
    u16* xs = (u16*)(smem + 64 * 66 * 2);
    const int s0 = (b & 31) * 64, d0 = (b >> 5) * 64;
    const int c = t & 7, r = t >> 3, s = s0 + r;
    u32x4 wv[4], kr[4];
#pragma unroll
    for (int i = 0; i < 4; ++i) wv[i] = *(const u32x4*)(shk + (d0 + 8 * c) * 4 + 8 * i);
#pragma unroll
    for (int j = 0; j < 4; ++j) {
      const int ss = s - 3 + j;
      u32x4 z = {0u, 0u, 0u, 0u};
      kr[j] = (ss >= 0) ? *(const u32x4*)(k + ss * D_ + d0 + 8 * c) : z;
    }
    const u32x4 vv = *(const u32x4*)(v + s * D_ + d0 + 8 * c);
    const u32x4 qq = *(const u32x4*)(q + s * D_ + d0 + 8 * c);
#pragma unroll
    for (int p = 0; p < 4; ++p) {
      const f32x2 w0 = {bflo(wv[p][0]), bflo(wv[p][2])}, w1 = {bfhi(wv[p][0]), bfhi(wv[p][2])};
      const f32x2 w2 = {bflo(wv[p][1]), bflo(wv[p][3])}, w3 = {bfhi(wv[p][1]), bfhi(wv[p][3])};
      f32x2 ks = rbf2(unpk(kr[0][p]) * w3);
      ks = rbf2(ks + rbf2(unpk(kr[1][p]) * w2));
      ks = rbf2(ks + rbf2(unpk(kr[2][p]) * w1));
      ks = rbf2(ks + rbf2(unpk(kr[3][p]) * w0));
      const unsigned kvb = pk2(ks * unpk(vv[p]));
      kvs[(8 * c + 2 * p) * 66 + (63 - r)] = (u16)kvb;
      kvs[(8 * c + 2 * p + 1) * 66 + (63 - r)] = (u16)(kvb >> 16);
      xs[(8 * c + 2 * p) * 66 + r] = (u16)qq[p];
      xs[(8 * c + 2 * p + 1) * 66 + r] = (u16)(qq[p] >> 16);
    }
    __syncthreads();
    {
      const int ch = r;
      const unsigned* a = (const unsigned*)(kvs + ch * 66 + 8 * c);
      const unsigned* bq = (const unsigned*)(xs + ch * 66 + 8 * c);
      u32x4 o1 = {a[0], a[1], a[2], a[3]};
      u32x4 o2 = {bq[0], bq[1], bq[2], bq[3]};
      *(u32x4*)(kvR + (d0 + ch) * S_ + (1984 - s0) + 8 * c) = o1;
      *(u32x4*)(xT + (d0 + ch) * S_ + s0 + 8 * c) = o2;
    }
  } else {
    const int lane = t & 63, w = t >> 6;
#pragma unroll
    for (int i = 0; i < 2; ++i) {
      const int row = (b - 256) * 16 + w * 2 + i;
      const u32x4 x = *(const u32x4*)(q + row * D_ + lane * 8);
      f32x2 f[4];
#pragma unroll
      for (int j = 0; j < 4; ++j) f[j] = unpk(x[j]);
      f32x2 s2 = (f[0] + f[1]) + (f[2] + f[3]);
      float sum = s2[0] + s2[1];
#pragma unroll
      for (int o = 32; o >= 1; o >>= 1) sum += __shfl_xor(sum, o);
      const float mean = sum * (1.0f / D_);
      f32x2 q2 = {0.f, 0.f};
#pragma unroll
      for (int j = 0; j < 4; ++j) { const f32x2 d = f[j] - mean; q2 += d * d; }
      float sq = q2[0] + q2[1];
#pragma unroll
      for (int o = 32; o >= 1; o >>= 1) sq += __shfl_xor(sq, o);
      const float var = rbf(sq * (1.0f / D_));
      if (lane == 0) {
        qm[row] = rbf(mean);
        qinv[row] = 1.0f / rbf(sqrtf(rbf(var + 1e-5f)));
      }
    }
  }
}

static constexpr int K2_COPY = 4160;
static constexpr int K2_KC = 8 * K2_COPY;
static constexpr int K2_XR = 96 * 80;
static constexpr int K2_CH = K2_KC + K2_XR;

__global__ __launch_bounds__(512) void k2_conv(const u16* __restrict__ ssmk, const u16* __restrict__ ssmD,
                                               const u16* __restrict__ kvR, const u16* __restrict__ xT,
                                               const float* __restrict__ qm, const float* __restrict__ qinv,
                                               u16* __restrict__ hT,
                                               const u16* __restrict__ wg, const u16* __restrict__ wu,
                                               u16* __restrict__ wp, float* __restrict__ csp,
                                               const u16* __restrict__ wd, unsigned* __restrict__ sink) {
  __shared__ __attribute__((aligned(16))) unsigned char smem[2 * K2_CH];
  const int t = threadIdx.x, lane = t & 63, w = t >> 6;
  if (blockIdx.x >= 384) {
    const u32x4* src = (const u32x4*)wd + (blockIdx.x - 384) * 4096;
    unsigned a = 0;
#pragma unroll
    for (int i = 0; i < 8; ++i) { const u32x4 x = src[i * 512 + t]; a |= x[0] ^ x[1] ^ x[2] ^ x[3]; }
    if (a == 0x9e3779b9u) sink[t] = a;
    return;
  }
  if (blockIdx.x >= 256) {
    lds_u8* tile = (lds_u8*)smem;
    float* red = (float*)(smem + 28672);
    const int bb = blockIdx.x - 256, ft = bb & 15, kc = bb >> 4;
    const int part = t % 28, rg = t / 28;
    if (rg < 16) {
      f32x2 acc[4] = {{0.f, 0.f}, {0.f, 0.f}, {0.f, 0.f}, {0.f, 0.f}};
      const int mat = part >= 14;
      const u16* src = (mat ? wu : wg) + ft * 112 + (part - 14 * mat) * 8;
#pragma unroll
      for (int i = 0; i < 4; ++i) {
        const int kk = rg * 4 + i;
        const u32x4 x = *(const u32x4*)(src + (kc * 64 + kk) * F_);
        *(u32x4*)(smem + kk * 448 + part * 16) = x;
#pragma unroll
        for (int j = 0; j < 4; ++j) acc[j] += unpk(x[j]);
      }
      *(f32x4*)(red + rg * 224 + part * 8) = (f32x4){acc[0][0], acc[0][1], acc[1][0], acc[1][1]};
      *(f32x4*)(red + rg * 224 + part * 8 + 4) = (f32x4){acc[2][0], acc[2][1], acc[3][0], acc[3][1]};
    }
    __syncthreads();
    const int woff = (8 * (lane >> 5) + ((lane & 15) >> 2)) * 448 + ((lane >> 4) & 1) * 224 + (lane & 3) * 8;
#pragma unroll 1
    for (int f = w; f < 28; f += 8) {
      const int j = f >> 2, ks = f & 3;
      const bf16x8 fr = tr_frag(tile + woff + ks * 16 * 448 + j * 32, 4 * 448);
      *(bf16x8*)(wp + ((((size_t)ft * 7 + j) * 32 + kc * 4 + ks) * 64 + lane) * 8) = fr;
    }
    if (t < 224) {
      float s = 0.f;
#pragma unroll
      for (int i = 0; i < 16; ++i) s += red[i * 224 + t];
      csp[(kc * 16 + ft) * 224 + t] = s;
    }
    return;
  }
  const int d0 = blockIdx.x * 2;
  const u32x4 z4 = {0u, 0u, 0u, 0u};
  const int ch = w >> 2, qq = w & 3;
  const int il = lane & 7, ih = (lane >> 3) & 3, hh = lane >> 5, col = lane & 31;
  const int d = d0 + ch;
  STAMP(1,0);
  const int tv = t & 255;
  const u16* kk = ssmk + d * S_;
  u32x4 cur = *(const u32x4*)(kk + tv * 8);
  u32x4 nxt = (tv < 255) ? *(const u32x4*)(kk + tv * 8 + 8) : z4;
  const u32x4 xv = *(const u32x4*)(kvR + d * S_ + tv * 8);
  const int nb0 = 1024 * (qq >> 1) + 32 * col + 16 * (qq & 1) + 4 * hh;
  u32x2 xq[2];
  f32x4 mm[2], dd[2];
#pragma unroll
  for (int e = 0; e < 2; ++e) {
    xq[e] = *(const u32x2*)(xT + d * S_ + nb0 + 8 * e);
    mm[e] = *(const f32x4*)(qm + nb0 + 8 * e);
    dd[e] = *(const f32x4*)(qinv + nb0 + 8 * e);
  }
  const float Dd = bflo(ssmD[d]);
  unsigned char* base = smem + ch * K2_CH;
  *(u32x4*)(base + K2_KC + (tv >> 2) * 80 + (tv & 3) * 16) = xv;
  if (tv < 128) *(u32x4*)(base + K2_KC + (64 + (tv >> 2)) * 80 + (tv & 3) * 16) = z4;
  int unit = tv + 4;
#pragma unroll 1
  for (int rep = 0; rep < 2; ++rep) {
    const unsigned wd[8] = {cur[0], cur[1], cur[2], cur[3], nxt[0], nxt[1], nxt[2], nxt[3]};
#pragma unroll
    for (int sh = 1; sh <= 8; ++sh) {
      u32x4 o;
      const int a = sh >> 1;
#pragma unroll
      for (int i = 0; i < 4; ++i) o[i] = (sh & 1) ? __builtin_amdgcn_alignbit(wd[a + i + 1], wd[a + i], 16) : wd[a + i];
      *(u32x4*)(base + (sh - 1) * K2_COPY + unit * 16) = o;
    }
    if (tv >= 4) break;
    nxt = (tv == 0) ? cur : z4;
    cur = z4;
    unit = (tv + 3) & 3;
  }
  STAMP(1,1);
  __syncthreads();
  STAMP(1,2);
  const unsigned char* kcb = base + il * K2_COPY + (2 + ih + hh + 32 * qq) * 16;
  const unsigned char* xb = base + K2_KC + (63 - col + 8 * qq) * 80 + (16 + 8 * hh) * 2;
  f32x16 acc0, acc1;
#pragma unroll
  for (int i = 0; i < 16; ++i) { acc0[i] = 0.f; acc1[i] = 0.f; }
#pragma unroll 2
  for (int c = 0; c < 8; ++c) {
#pragma unroll
    for (int s = 0; s < 2; ++s) {
      const bf16x8 a0 = *(const bf16x8*)(kcb + (4 * c - 2 * s) * 16);
      const bf16x8 a1 = *(const bf16x8*)(kcb + (4 * c - 2 * s + 128) * 16);
      const bf16x8 b0 = *(const bf16x8*)(xb + c * 80 - 32 * s);
      const bf16x8 b1 = *(const bf16x8*)(xb + (c - 32) * 80 - 32 * s);
      acc0 = __builtin_amdgcn_mfma_f32_32x32x16_bf16(a0, b0, acc0, 0, 0, 0);
      acc1 = __builtin_amdgcn_mfma_f32_32x32x16_bf16(a0, b1, acc1, 0, 0, 0);
      acc1 = __builtin_amdgcn_mfma_f32_32x32x16_bf16(a1, b0, acc1, 0, 0, 0);
    }
  }
  STAMP(1,3);
  __syncthreads();
  f32x4* red = (f32x4*)base;
#pragma unroll
  for (int g = 0; g < 4; ++g) {
    red[(qq * 8 + g) * 64 + lane] = (f32x4){acc0[4 * g], acc0[4 * g + 1], acc0[4 * g + 2], acc0[4 * g + 3]};
    red[(qq * 8 + 4 + g) * 64 + lane] = (f32x4){acc1[4 * g], acc1[4 * g + 1], acc1[4 * g + 2], acc1[4 * g + 3]};
  }
  __syncthreads();
  STAMP(1,4);
  unsigned pk[2][2];
#pragma unroll
  for (int e = 0; e < 2; ++e) {
    const f32x4* rp = red + (2 * qq + e) * 64 + lane;
    const f32x4 y4 = (rp[0] + rp[512]) + (rp[1024] + rp[1536]);
    const int t0 = 2044 - (nb0 + 8 * e);
    const u32x2 kvp = *(const u32x2*)(base + K2_KC + (t0 >> 5) * 80 + (t0 & 31) * 2);
#pragma unroll
    for (int r = 0; r < 2; ++r) {
      const f32x2 kvf = {bfhi(kvp[1 - r]), bflo(kvp[1 - r])};
      const f32x2 xf = unpk(xq[e][r]);
      const f32x2 yy = rbf2((f32x2){y4[2 * r], y4[2 * r + 1]});
      const f32x2 y2 = rbf2(yy + rbf2(kvf * Dd));
      const f32x2 qn = rbf2(rbf2(xf - (f32x2){mm[e][2 * r], mm[e][2 * r + 1]}) * (f32x2){dd[e][2 * r], dd[e][2 * r + 1]});
      pk[e][r] = pk2(xf + rbf2(qn * y2));
    }
  }
  auto s0 = __builtin_amdgcn_permlane32_swap(pk[0][0], pk[1][0], false, false);
  auto s1 = __builtin_amdgcn_permlane32_swap(pk[0][1], pk[1][1], false, false);
  const u32x4 ov = {s0[0], s1[0], s0[1], s1[1]};
  STAMP(1,5);
  *(u32x4*)(hT + d * S_ + 1024 * (qq >> 1) + 32 * col + 16 * (qq & 1) + 8 * hh) = ov;
  STAMP(1,6);
  DRAIN();
  STAMP(1,7);
}

DEV void gload16(bf16x8& dst, const void* sbase, unsigned voff) {
  asm volatile("global_load_dwordx4 %0, %1, %2" : "=v"(dst) : "v"(voff), "s"(sbase) : "memory");
}

template <bool STATS>
DEV void k3_main(lds_u8* smem, const u16* wp, int ft, int w, int lane, float* stl, f32x16* acc) {
  const int hf = lane >> 5, q4 = (lane & 15) >> 2;
  {
    const int bsl = 2 * ((lane >> 4) & 1) + ((lane & 3) >> 1);
    int boff[4];
#pragma unroll
    for (int i = 0; i < 4; ++i)
      boff[i] = (8 * hf + q4) * 256 + (((4 * ((i + w) & 3) + bsl) ^ (q4 << 2)) * 16) + (lane & 1) * 8;
    const char* wbase = (const char*)(wp + ((size_t)ft * 7 + w) * 32 * 512);
    const unsigned voffW = lane * 16;
    bf16x8 wf[16];
#pragma unroll
    for (int i = 0; i < 12; ++i) gload16(wf[i], wbase + i * 1024, voffW);
#pragma unroll
    for (int j = 0; j < 4; ++j)
#pragma unroll
      for (int i = 0; i < 16; ++i) acc[j][i] = 0.f;
    float sum0 = 0.f, sum1 = 0.f, sq0 = 0.f, sq1 = 0.f;
    bf16x8 b0[4], b1[4];
    const lds_u8* sm = smem;
    auto ld = [&](int kg, bf16x8* bb) {
#pragma unroll
      for (int i = 0; i < 4; ++i) bb[i] = tr_frag(sm + kg * 4096 + boff[i], 1024);
    };
    auto mm = [&](int kg, const bf16x8* bb) {
#pragma unroll
      for (int i = 0; i < 4; ++i) acc[i] = __builtin_amdgcn_mfma_f32_32x32x16_bf16(wf[kg], bb[i], acc[i], 0, 0, 0);
      if (STATS) {
        const u32x4 bu = __builtin_bit_cast(u32x4, bb[0]);
#pragma unroll
        for (int i = 0; i < 4; ++i) {
          const unsigned xi = bu[i];
          const float lo = bflo(xi), hi = bfhi(xi);
          sum0 += lo; sum1 += hi;
          sq0 = fmaf(lo, lo, sq0); sq1 = fmaf(hi, hi, sq1);
        }
        asm volatile("" : "+v"(sum0), "+v"(sum1), "+v"(sq0), "+v"(sq1));
      }
    };
    STAMP(2,1);
    asm volatile("s_waitcnt vmcnt(8)" : "+v"(wf[0]), "+v"(wf[1]), "+v"(wf[2]), "+v"(wf[3]) :: "memory");
    __builtin_amdgcn_s_barrier();
    STAMP(2,2);
    ld(0, b0);
#pragma unroll 1
    for (int half = 0; half < 2; ++half) {
#pragma unroll
      for (int cc = 0; cc < 4; ++cc) {
        if (half == 0 || cc == 0) {
#pragma unroll
          for (int i = 0; i < 4; ++i) gload16(wf[4 * ((cc + 3) & 3) + i], wbase + (4 * cc + 12 + i) * 1024, voffW);
        }
        ld(4 * cc + 1, b1); mm(4 * cc, b0);
        ld(4 * cc + 2, b0); mm(4 * cc + 1, b1);
        ld(4 * cc + 3, b1); mm(4 * cc + 2, b0);
        if (half == 0 || cc < 3) {
          const int ns = (cc + 1) & 3;
          if (half == 0 || cc == 0)
            asm volatile("s_waitcnt vmcnt(8)" : "+v"(wf[4 * ns]), "+v"(wf[4 * ns + 1]), "+v"(wf[4 * ns + 2]), "+v"(wf[4 * ns + 3]) :: "memory");
          else if (cc == 1)
            asm volatile("s_waitcnt vmcnt(4)" : "+v"(wf[4 * ns]), "+v"(wf[4 * ns + 1]), "+v"(wf[4 * ns + 2]), "+v"(wf[4 * ns + 3]) :: "memory");
          else
            asm volatile("s_waitcnt vmcnt(0)" : "+v"(wf[4 * ns]), "+v"(wf[4 * ns + 1]), "+v"(wf[4 * ns + 2]), "+v"(wf[4 * ns + 3]) :: "memory");
          __builtin_amdgcn_s_barrier();
          ld(4 * cc + 4, b0);
        }
        mm(4 * cc + 3, b1);
      }
      sm += 65536;
      wbase += 16384;
    }
    STAMP(2,5);
    if (STATS) {
      float sum = sum0 + sum1, sq = sq0 + sq1;
      sum += __shfl_xor(sum, 32);
      sq += __shfl_xor(sq, 32);
      const float mean = sum * (1.0f / D_);
      const float var = rbf(fmaxf(sq * (1.0f / D_) - mean * mean, 0.f));
      const float inv_ = 1.0f / rbf(sqrtf(rbf(var + 1e-5f)));
      if (lane < 32) *(f32x2*)(stl + (w * 32 + lane) * 2) = (f32x2){inv_, -rbf(mean) * inv_};
    }
  }
}

__global__ __launch_bounds__(512) void k3_gemm1(const u16* __restrict__ hT, const u16* __restrict__ wp,
                                                const float* __restrict__ csp, u16* __restrict__ hh,
                                                const u16* __restrict__ wd) {
  __shared__ __attribute__((aligned(16))) unsigned char smem_[131072 + 2048 + 7168];
  lds_u8* smem = (lds_u8*)smem_;
  const unsigned sbase = (unsigned)(size_t)smem;
  const int t = threadIdx.x, lane = t & 63;
  const int w = __builtin_amdgcn_readfirstlane(t >> 6);
  const int b = blockIdx.x, x = b & 7, idx = b >> 3;
  const int mt = (x & 1) * 8 + (idx & 7), ft = (x >> 1) * 4 + (idx >> 3);
  const int m0 = mt * 128, f0 = ft * 112;
  STAMP(2,0);
  float* csl = (float*)(smem_ + 131072);
  float* stl = csl + 224;
  const int hf = lane >> 5;
  f32x16 acc[4];
  if (w < 4) {
    k3_main<true>(smem, wp, ft, w, lane, stl, acc);
  } else if (w < 7) {
    k3_main<false>(smem, wp, ft, w, lane, stl, acc);
  } else {
    const unsigned voffA = ((lane >> 4) * S_ + (((lane & 15) ^ ((lane >> 4) << 2)) * 8)) * 2;
    const char* abase = (const char*)(hT + m0);
    auto issue = [&](int c) {
#pragma unroll
      for (int i = 0; i < 16; ++i) glds16(abase + (size_t)(c * 64 + 4 * i) * S_ * 2, voffA, sbase + c * 16384 + i * 1024);
    };
    bf16x8 cpart[8];
    const unsigned voffC = (lane < 56 ? lane : 55) * 16;
    issue(0);
#pragma unroll
    for (int k8 = 0; k8 < 8; ++k8) gload16(cpart[k8], (const char*)(csp + (k8 * 16 + ft) * 224), voffC);
    issue(1);
    const char* pf = (const char*)wd + idx * 57344;
    auto prefetch = [&](int p0, int n) {
      for (int i = 0; i < n; ++i) glds16(pf + (p0 + i) * 1024, lane * 16, sbase + 131072 + 4096);
    };
#pragma unroll 1
    for (int c = 0; c < 8; ++c) {
      if (c == 0) WAITV(16); else if (c < 6) WAITV(32); else if (c == 6) WAITV(30); else WAITV(28);
      __builtin_amdgcn_s_barrier();
      if (c == 0) issue(2);
      if (c < 5) issue(c + 3);
      if (c == 5 || c == 6) prefetch((c - 5) * 14, 14);
    }
    prefetch(28, 28);
    WAITV(0);
    asm volatile("" : "+v"(cpart[0]), "+v"(cpart[1]), "+v"(cpart[2]), "+v"(cpart[3]),
                      "+v"(cpart[4]), "+v"(cpart[5]), "+v"(cpart[6]), "+v"(cpart[7]));
    f32x4 cs4 = __builtin_bit_cast(f32x4, cpart[0]);
#pragma unroll
    for (int k8 = 1; k8 < 8; ++k8) cs4 += __builtin_bit_cast(f32x4, cpart[k8]);
    if (lane < 56) *(f32x4*)(csl + lane * 4) = cs4;
  }
  __syncthreads();
  if (w < 7) {
    f32x4 cg[2], cu[2];
#pragma unroll
    for (int g = 0; g < 2; ++g) {
      cg[g] = *(const f32x4*)(csl + 16 * w + 8 * g + 4 * hf);
      cu[g] = *(const f32x4*)(csl + 112 + 16 * w + 8 * g + 4 * hf);
    }
    u16* orow = hh + (size_t)(m0 + (lane & 31)) * F_ + f0 + 16 * w + 8 * hf;
#pragma unroll
    for (int i = 0; i < 4; ++i) {
      const int mb = (i + w) & 3;
      const f32x2 st2 = *(const f32x2*)(stl + (mb * 32 + (lane & 31)) * 2);
      const float inv = st2[0], mi = st2[1];
      unsigned o[4];
#pragma unroll
      for (int g = 0; g < 2; ++g) {
#pragma unroll
        for (int r = 0; r < 2; ++r) {
          const f32x2 ag = {acc[i][4 * g + 2 * r], acc[i][4 * g + 2 * r + 1]};
          const f32x2 au = {acc[i][8 + 4 * g + 2 * r], acc[i][8 + 4 * g + 2 * r + 1]};
          f32x2 gt = ag * inv + (f32x2){cg[g][2 * r], cg[g][2 * r + 1]} * mi;
          const f32x2 up = au * inv + (f32x2){cu[g][2 * r], cu[g][2 * r + 1]} * mi;
          gt[0] = gt[0] > 0.f ? gt[0] : 0.f;
          gt[1] = gt[1] > 0.f ? gt[1] : 0.f;
          o[2 * g + r] = pk2(gt * up);
        }
      }
      auto s0 = __builtin_amdgcn_permlane32_swap(o[0], o[2], false, false);
      auto s1 = __builtin_amdgcn_permlane32_swap(o[1], o[3], false, false);
      u32x4 ov = {s0[0], s1[0], s0[1], s1[1]};
      *(u32x4*)(orow + (size_t)mb * 32 * F_) = ov;
    }
  }
  STAMP(2,6);
  DRAIN();
  STAMP(2,7);
}

static constexpr int K4_STAGE = 32768;

__global__ __launch_bounds__(384) void k4_gemm2(const u16* __restrict__ hh, const u16* __restrict__ wd,
                                                const u16* __restrict__ hT, u16* __restrict__ out) {
  __shared__ __attribute__((aligned(16))) unsigned char smem_[4 * K4_STAGE];
  lds_u8* smem = (lds_u8*)smem_;
  const int t = threadIdx.x, lane = t & 63;
  const int w = __builtin_amdgcn_readfirstlane(t >> 6);
  const int b = blockIdx.x, x = b & 7, idx = b >> 3;
  const int mt = x * 4 + (idx & 3), nt = idx >> 2;
  const int m0 = mt * 64, n0 = nt * 64;
  STAMP(3,0);
  const int hf = lane >> 5, r32 = lane & 31;
  if (w >= 4) {
    const unsigned sbase = (unsigned)(size_t)smem + (w - 4) * 16384;
    unsigned voff[4];
    const char* base;
    size_t pstride, sstride;
    if (w == 4) {
      const int ar = lane >> 4, as = lane & 15;
#pragma unroll
      for (int i = 0; i < 4; ++i) voff[i] = ((4 * i + ar) * F_ + (as ^ (4 * i + ar)) * 8) * 2;
      base = (const char*)(hh + (size_t)m0 * F_);
      pstride = (size_t)16 * F_ * 2; sstride = 256;
    } else {
      const int br = lane >> 3, bs = lane & 7;
#pragma unroll
      for (int i = 0; i < 4; ++i) voff[i] = ((8 * i + br) * D_ + (bs ^ (((br >> 1) & 1) << 2)) * 8) * 2;
      base = (const char*)(wd + n0);
      pstride = (size_t)32 * D_ * 2; sstride = (size_t)128 * D_ * 2;
    }
    auto issue = [&](int st) {
      const unsigned sb = sbase + (st & 3) * K4_STAGE;
      const char* pp = base + st * sstride;
#pragma unroll
      for (int i = 0; i < 16; ++i) glds16(pp + (i >> 2) * pstride, voff[i & 3], sb + i * 1024);
    };
    issue(0); issue(1);
#pragma unroll 1
    for (int st = 0; st < 14; ++st) {
      if (st == 0 || st == 12) WAITV(16); else if (st < 12) WAITV(32); else WAITV(0);
      __builtin_amdgcn_s_barrier();
      if (st == 0) issue(2);
      if (st + 3 < 14) issue(st + 3);
    }
  } else {
    const int mb = w >> 1, nb = w & 1;
    u32x2 hr[4];
#pragma unroll
    for (int g = 0; g < 4; ++g) hr[g] = *(const u32x2*)(hT + (n0 + 32 * nb + r32) * S_ + m0 + 32 * mb + 8 * g + 4 * hf);
    f32x16 acc;
#pragma unroll
    for (int e = 0; e < 16; ++e) acc[e] = 0.f;
    const int bq = (lane & 15) >> 2;
    const int bsw = ((bq >> 1) & 1) << 2;
    const int bcol = 32 * nb + 16 * ((lane >> 4) & 1) + 4 * (lane & 3);
    const int boff = 16384 + (8 * hf + bq) * 128 + (((bcol >> 3) ^ bsw) * 16) + (bcol & 7) * 2;
    const int arow = (32 * mb + r32) * 256;
    const int asw = r32 & 15;
#pragma unroll 1
    for (int st = 0; st < 14; ++st) {
      if (st == 0) STAMP(3,1);
      __builtin_amdgcn_s_barrier();
      if (st == 0) STAMP(3,2);
      if (st == 1) STAMP(3,3);
      if (st == 7) STAMP(3,4);
      const lds_u8* sb = smem + (st & 3) * K4_STAGE;
      bf16x8 af[8], bf[8];
#pragma unroll
      for (int ks = 0; ks < 8; ++ks) {
        af[ks] = *(const __attribute__((address_space(3))) bf16x8*)(sb + arow + (((2 * ks + hf) ^ asw) * 16));
        bf[ks] = tr_frag(sb + boff + ks * 16 * 128, 4 * 128);
      }
#pragma unroll
      for (int ks = 0; ks < 8; ++ks) acc = __builtin_amdgcn_mfma_f32_32x32x16_bf16(af[ks], bf[ks], acc, 0, 0, 0);
    }
    STAMP(3,5);
    u16* ob = (u16*)(smem_ + 2 * K4_STAGE + w * 4096);
#pragma unroll
    for (int g = 0; g < 4; ++g) {
      const unsigned o0 = pk2(unpk(hr[g][0]) + rbf2((f32x2){acc[4 * g], acc[4 * g + 1]}));
      const unsigned o1 = pk2(unpk(hr[g][1]) + rbf2((f32x2){acc[4 * g + 2], acc[4 * g + 3]}));
      u16* op = ob + (8 * g + 4 * hf) * 40 + r32;
      op[0] = (u16)o0; op[40] = (u16)(o0 >> 16); op[80] = (u16)o1; op[120] = (u16)(o1 >> 16);
    }
    __builtin_amdgcn_wave_barrier();
    asm volatile("s_waitcnt lgkmcnt(0)" ::: "memory");
    const int row = lane >> 1, hc = (lane & 1) * 16;
    const u32x4 v0 = *(const u32x4*)(ob + row * 40 + hc);
    const u32x4 v1 = *(const u32x4*)(ob + row * 40 + hc + 8);
    u16* dst = out + (m0 + 32 * mb + row) * D_ + n0 + 32 * nb + hc;
    *(u32x4*)(dst) = v0;
    *(u32x4*)(dst + 8) = v1;
    STAMP(3,6);
    DRAIN();
    STAMP(3,7);
  }
}

extern "C" void kernel_launch(void* const* d_in, const int* in_sizes, int n_in, void* d_out, int out_size,
                              void* d_ws, size_t ws_size, hipStream_t stream) {
  const u16* q = (const u16*)d_in[0];
  const u16* k = (const u16*)d_in[1];
  const u16* v = (const u16*)d_in[2];
  const u16* shk = (const u16*)d_in[3];
  const u16* ssmk = (const u16*)d_in[4];
  const u16* ssmD = (const u16*)d_in[5];
  const u16* wg = (const u16*)d_in[7];
  const u16* wu = (const u16*)d_in[8];
  const u16* wd = (const u16*)d_in[9];
  unsigned char* ws = (unsigned char*)d_ws;
  u16* kvR = (u16*)(ws);
  u16* xT = (u16*)(ws + (2u << 20));
  float* qm = (float*)(ws + (4u << 20));
  float* qinv = qm + 2048;
  float* csp = (float*)(ws + (4u << 20) + 65536);
  u16* h = (u16*)(ws + (5u << 20));
  u16* hh = (u16*)(ws + (8u << 20));
  u16* wp = (u16*)(ws + (16u << 20));
  k1_prep<<<384, 512, 0, stream>>>(q, k, v, shk, kvR, xT, qm, qinv);
  k2_conv<<<412, 512, 0, stream>>>(ssmk, ssmD, kvR, xT, qm, qinv, h, wg, wu, wp, csp, wd, (unsigned*)(ws + (24u << 20)));
  k3_gemm1<<<256, 512, 0, stream>>>(h, wp, csp, hh, wd);
  k4_gemm2<<<256, 384, 0, stream>>>(hh, wd, h, (u16*)d_out);
}
```

```cpp
#include <hip/hip_runtime.h>
#include <hip/hip_bf16.h>
#include <stdint.h>

typedef unsigned short u16;
typedef __attribute__((ext_vector_type(8))) short bf16x8;
typedef __attribute__((ext_vector_type(4))) short s16x4;
typedef __attribute__((ext_vector_type(16))) float f32x16;
typedef __attribute__((ext_vector_type(4))) float f32x4;
typedef __attribute__((ext_vector_type(2))) float f32x2;
typedef __attribute__((ext_vector_type(4))) unsigned u32x4;
typedef __attribute__((ext_vector_type(2))) unsigned u32x2;
typedef __attribute__((ext_vector_type(2))) __bf16 bf16x2_t;
typedef __attribute__((ext_vector_type(4))) short v4i16_t;
typedef __attribute__((address_space(3))) unsigned char lds_u8;

#define DEV __device__ __forceinline__

static constexpr int S_ = 2048, D_ = 512, F_ = 1792;

DEV float bflo(unsigned x) { return __uint_as_float(x << 16); }
DEV float bfhi(unsigned x) { return __uint_as_float(x & 0xffff0000u); }
DEV f32x2 unpk(unsigned x) { return (f32x2){bflo(x), bfhi(x)}; }
DEV unsigned pk2(f32x2 v) {
  bf16x2_t r = {(__bf16)v[0], (__bf16)v[1]};
  return __builtin_bit_cast(unsigned, r);
}
DEV f32x2 rbf2(f32x2 v) { return unpk(pk2(v)); }
DEV float rbf(float x) { return (float)(__bf16)x; }

DEV void glds16(const void* sbase, unsigned voff, unsigned lds_dst) {
  asm volatile("s_mov_b32 m0, %2\n\ts_nop 0\n\tglobal_load_lds_dwordx4 %0, %1"
               :: "v"(voff), "s"(sbase), "s"(lds_dst) : "memory");
}
#define WAITV(n) asm volatile("s_waitcnt vmcnt(" #n ")" ::: "memory")

DEV s16x4 tr_read(const lds_u8* p) {
  return __builtin_bit_cast(s16x4, __builtin_amdgcn_ds_read_tr16_b64_v4i16((__attribute__((address_space(3))) v4i16_t*)p));
}
DEV bf16x8 tr_frag(const lds_u8* p, int rowstride4) {
  const s16x4 lo = tr_read(p);
  const s16x4 hi = tr_read(p + rowstride4);
  return (bf16x8){lo[0], lo[1], lo[2], lo[3], hi[0], hi[1], hi[2], hi[3]};
}

__global__ __launch_bounds__(512) void k1_prep(const u16* __restrict__ q, const u16* __restrict__ k,
                                               const u16* __restrict__ v, const u16* __restrict__ shk,
                                               u16* __restrict__ kvR, u16* __restrict__ xT,
                                               float* __restrict__ qm, float* __restrict__ qinv) {
  __shared__ __attribute__((aligned(16))) unsigned char smem[2 * 64 * 66 * 2];
  const int b = blockIdx.x, t = threadIdx.x;
  if (b < 256) {
    u16* kvs = (u16*)smem;
    u16* xs = (u16*)(smem + 64 * 66 * 2);
    const int s0 = (b & 31) * 64, d0 = (b >> 5) * 64;
    const int c = t & 7, r = t >> 3, s = s0 + r;
    u32x4 wv[4], kr[4];
#pragma unroll
    for (int i = 0; i < 4; ++i) wv[i] = *(const u32x4*)(shk + (d0 + 8 * c) * 4 + 8 * i);
#pragma unroll
    for (int j = 0; j < 4; ++j) {
      const int ss = s - 3 + j;
      u32x4 z = {0u, 0u, 0u, 0u};
      kr[j] = (ss >= 0) ? *(const u32x4*)(k + ss * D_ + d0 + 8 * c) : z;
    }
    const u32x4 vv = *(const u32x4*)(v + s * D_ + d0 + 8 * c);
    const u32x4 qq = *(const u32x4*)(q + s * D_ + d0 + 8 * c);
#pragma unroll
    for (int p = 0; p < 4; ++p) {
      const f32x2 w0 = {bflo(wv[p][0]), bflo(wv[p][2])}, w1 = {bfhi(wv[p][0]), bfhi(wv[p][2])};
      const f32x2 w2 = {bflo(wv[p][1]), bflo(wv[p][3])}, w3 = {bfhi(wv[p][1]), bfhi(wv[p][3])};
      f32x2 ks = rbf2(unpk(kr[0][p]) * w3);
      ks = rbf2(ks + rbf2(unpk(kr[1][p]) * w2));
      ks = rbf2(ks + rbf2(unpk(kr[2][p]) * w1));
      ks = rbf2(ks + rbf2(unpk(kr[3][p]) * w0));
      const unsigned kvb = pk2(ks * unpk(vv[p]));
      kvs[(8 * c + 2 * p) * 66 + (63 - r)] = (u16)kvb;
      kvs[(8 * c + 2 * p + 1) * 66 + (63 - r)] = (u16)(kvb >> 16);
      xs[(8 * c + 2 * p) * 66 + r] = (u16)qq[p];
      xs[(8 * c + 2 * p + 1) * 66 + r] = (u16)(qq[p] >> 16);
    }
    __syncthreads();
    {
      const int ch = r;
      const unsigned* a = (const unsigned*)(kvs + ch * 66 + 8 * c);
      const unsigned* bq = (const unsigned*)(xs + ch * 66 + 8 * c);
      u32x4 o1 = {a[0], a[1], a[2], a[3]};
      u32x4 o2 = {bq[0], bq[1], bq[2], bq[3]};
      *(u32x4*)(kvR + (d0 + ch) * S_ + (1984 - s0) + 8 * c) = o1;
      *(u32x4*)(xT + (d0 + ch) * S_ + s0 + 8 * c) = o2;
    }
  } else {
    const int lane = t & 63, w = t >> 6;
#pragma unroll
    for (int i = 0; i < 2; ++i) {
      const int row = (b - 256) * 16 + w * 2 + i;
      const u32x4 x = *(const u32x4*)(q + row * D_ + lane * 8);
      f32x2 f[4];
#pragma unroll
      for (int j = 0; j < 4; ++j) f[j] = unpk(x[j]);
      f32x2 s2 = (f[0] + f[1]) + (f[2] + f[3]);
      float sum = s2[0] + s2[1];
#pragma unroll
      for (int o = 32; o >= 1; o >>= 1) sum += __shfl_xor(sum, o);
      const float mean = sum * (1.0f / D_);
      f32x2 q2 = {0.f, 0.f};
#pragma unroll
      for (int j = 0; j < 4; ++j) { const f32x2 d = f[j] - mean; q2 += d * d; }
      float sq = q2[0] + q2[1];
#pragma unroll
      for (int o = 32; o >= 1; o >>= 1) sq += __shfl_xor(sq, o);
      const float var = rbf(sq * (1.0f / D_));
      if (lane == 0) {
        qm[row] = rbf(mean);
        qinv[row] = 1.0f / rbf(sqrtf(rbf(var + 1e-5f)));
      }
    }
  }
}

static constexpr int K2_COPY = 4160;
static constexpr int K2_KC = 8 * K2_COPY;
static constexpr int K2_XR = 96 * 80;
static constexpr int K2_CH = K2_KC + K2_XR;

__global__ __launch_bounds__(512) void k2_conv(const u16* __restrict__ ssmk, const u16* __restrict__ ssmD,
                                               const u16* __restrict__ kvR, const u16* __restrict__ xT,
                                               const float* __restrict__ qm, const float* __restrict__ qinv,
                                               u16* __restrict__ hT,
                                               const u16* __restrict__ wg, const u16* __restrict__ wu,
                                               u16* __restrict__ wp, float* __restrict__ csp,
                                               const u16* __restrict__ wd, unsigned* __restrict__ sink) {
  __shared__ __attribute__((aligned(16))) unsigned char smem[2 * K2_CH];
  const int t = threadIdx.x, lane = t & 63, w = t >> 6;
  if (blockIdx.x >= 384) {
    const u32x4* src = (const u32x4*)wd + (blockIdx.x - 384) * 4096;
    unsigned a = 0;
#pragma unroll
    for (int i = 0; i < 8; ++i) { const u32x4 x = src[i * 512 + t]; a |= x[0] ^ x[1] ^ x[2] ^ x[3]; }
    if (a == 0x9e3779b9u) sink[t] = a;
    return;
  }
  if (blockIdx.x >= 256) {
    lds_u8* tile = (lds_u8*)smem;
    float* red = (float*)(smem + 28672);
    const int bb = blockIdx.x - 256, ft = bb & 15, kc = bb >> 4;
    const int part = t % 28, rg = t / 28;
    if (rg < 16) {
      f32x2 acc[4] = {{0.f, 0.f}, {0.f, 0.f}, {0.f, 0.f}, {0.f, 0.f}};
      const int mat = part >= 14;
      const u16* src = (mat ? wu : wg) + ft * 112 + (part - 14 * mat) * 8;
#pragma unroll
      for (int i = 0; i < 4; ++i) {
        const int kk = rg * 4 + i;
        const u32x4 x = *(const u32x4*)(src + (kc * 64 + kk) * F_);
        *(u32x4*)(smem + kk * 448 + part * 16) = x;
#pragma unroll
        for (int j = 0; j < 4; ++j) acc[j] += unpk(x[j]);
      }
      *(f32x4*)(red + rg * 224 + part * 8) = (f32x4){acc[0][0], acc[0][1], acc[1][0], acc[1][1]};
      *(f32x4*)(red + rg * 224 + part * 8 + 4) = (f32x4){acc[2][0], acc[2][1], acc[3][0], acc[3][1]};
    }
    __syncthreads();
    const int woff = (8 * (lane >> 5) + ((lane & 15) >> 2)) * 448 + ((lane >> 4) & 1) * 224 + (lane & 3) * 8;
#pragma unroll 1
    for (int f = w; f < 28; f += 8) {
      const int j = f >> 2, ks = f & 3;
      const bf16x8 fr = tr_frag(tile + woff + ks * 16 * 448 + j * 32, 4 * 448);
      *(bf16x8*)(wp + ((((size_t)ft * 7 + j) * 32 + kc * 4 + ks) * 64 + lane) * 8) = fr;
    }
    if (t < 224) {
      float s = 0.f;
#pragma unroll
      for (int i = 0; i < 16; ++i) s += red[i * 224 + t];
      csp[(kc * 16 + ft) * 224 + t] = s;
    }
    return;
  }
  const int d0 = blockIdx.x * 2;
  const u32x4 z4 = {0u, 0u, 0u, 0u};
  const int ch = w >> 2, qq = w & 3;
  const int il = lane & 7, ih = (lane >> 3) & 3, hh = lane >> 5, col = lane & 31;
  const int d = d0 + ch;
  const int tv = t & 255;
  const u16* kk = ssmk + d * S_;
  u32x4 cur = *(const u32x4*)(kk + tv * 8);
  u32x4 nxt = (tv < 255) ? *(const u32x4*)(kk + tv * 8 + 8) : z4;
  const u32x4 xv = *(const u32x4*)(kvR + d * S_ + tv * 8);
  const int nb0 = 1024 * (qq >> 1) + 32 * col + 16 * (qq & 1) + 4 * hh;
  u32x2 xq[2];
  f32x4 mm[2], dd[2];
#pragma unroll
  for (int e = 0; e < 2; ++e) {
    xq[e] = *(const u32x2*)(xT + d * S_ + nb0 + 8 * e);
    mm[e] = *(const f32x4*)(qm + nb0 + 8 * e);
    dd[e] = *(const f32x4*)(qinv + nb0 + 8 * e);
  }
  const float Dd = bflo(ssmD[d]);
  unsigned char* base = smem + ch * K2_CH;
  *(u32x4*)(base + K2_KC + (tv >> 2) * 80 + (tv & 3) * 16) = xv;
  if (tv < 128) *(u32x4*)(base + K2_KC + (64 + (tv >> 2)) * 80 + (tv & 3) * 16) = z4;
  int unit = tv + 4;
#pragma unroll 1
  for (int rep = 0; rep < 2; ++rep) {
    const unsigned wd[8] = {cur[0], cur[1], cur[2], cur[3], nxt[0], nxt[1], nxt[2], nxt[3]};
#pragma unroll
    for (int sh = 1; sh <= 8; ++sh) {
      u32x4 o;
      const int a = sh >> 1;
#pragma unroll
      for (int i = 0; i < 4; ++i) o[i] = (sh & 1) ? __builtin_amdgcn_alignbit(wd[a + i + 1], wd[a + i], 16) : wd[a + i];
      *(u32x4*)(base + (sh - 1) * K2_COPY + unit * 16) = o;
    }
    if (tv >= 4) break;
    nxt = (tv == 0) ? cur : z4;
    cur = z4;
    unit = (tv + 3) & 3;
  }
  __syncthreads();
  const unsigned char* kcb = base + il * K2_COPY + (2 + ih + hh + 32 * qq) * 16;
  const unsigned char* xb = base + K2_KC + (63 - col + 8 * qq) * 80 + (16 + 8 * hh) * 2;
  f32x16 acc0, acc1;
#pragma unroll
  for (int i = 0; i < 16; ++i) { acc0[i] = 0.f; acc1[i] = 0.f; }
#pragma unroll 2
  for (int c = 0; c < 8; ++c) {
#pragma unroll
    for (int s = 0; s < 2; ++s) {
      const bf16x8 a0 = *(const bf16x8*)(kcb + (4 * c - 2 * s) * 16);
      const bf16x8 a1 = *(const bf16x8*)(kcb + (4 * c - 2 * s + 128) * 16);
      const bf16x8 b0 = *(const bf16x8*)(xb + c * 80 - 32 * s);
      const bf16x8 b1 = *(const bf16x8*)(xb + (c - 32) * 80 - 32 * s);
      acc0 = __builtin_amdgcn_mfma_f32_32x32x16_bf16(a0, b0, acc0, 0, 0, 0);
      acc1 = __builtin_amdgcn_mfma_f32_32x32x16_bf16(a0, b1, acc1, 0, 0, 0);
      acc1 = __builtin_amdgcn_mfma_f32_32x32x16_bf16(a1, b0, acc1, 0, 0, 0);
    }
  }
  __syncthreads();
  f32x4* red = (f32x4*)base;
#pragma unroll
  for (int g = 0; g < 4; ++g) {
    red[(qq * 8 + g) * 64 + lane] = (f32x4){acc0[4 * g], acc0[4 * g + 1], acc0[4 * g + 2], acc0[4 * g + 3]};
    red[(qq * 8 + 4 + g) * 64 + lane] = (f32x4){acc1[4 * g], acc1[4 * g + 1], acc1[4 * g + 2], acc1[4 * g + 3]};
  }
  __syncthreads();
  unsigned pk[2][2];
#pragma unroll
  for (int e = 0; e < 2; ++e) {
    const f32x4* rp = red + (2 * qq + e) * 64 + lane;
    const f32x4 y4 = (rp[0] + rp[512]) + (rp[1024] + rp[1536]);
    const int t0 = 2044 - (nb0 + 8 * e);
    const u32x2 kvp = *(const u32x2*)(base + K2_KC + (t0 >> 5) * 80 + (t0 & 31) * 2);
#pragma unroll
    for (int r = 0; r < 2; ++r) {
      const f32x2 kvf = {bfhi(kvp[1 - r]), bflo(kvp[1 - r])};
      const f32x2 xf = unpk(xq[e][r]);
      const f32x2 yy = rbf2((f32x2){y4[2 * r], y4[2 * r + 1]});
      const f32x2 y2 = rbf2(yy + rbf2(kvf * Dd));
      const f32x2 qn = rbf2(rbf2(xf - (f32x2){mm[e][2 * r], mm[e][2 * r + 1]}) * (f32x2){dd[e][2 * r], dd[e][2 * r + 1]});
      pk[e][r] = pk2(xf + rbf2(qn * y2));
    }
  }
  auto s0 = __builtin_amdgcn_permlane32_swap(pk[0][0], pk[1][0], false, false);
  auto s1 = __builtin_amdgcn_permlane32_swap(pk[0][1], pk[1][1], false, false);
  const u32x4 ov = {s0[0], s1[0], s0[1], s1[1]};
  *(u32x4*)(hT + d * S_ + 1024 * (qq >> 1) + 32 * col + 16 * (qq & 1) + 8 * hh) = ov;
}

DEV void gload16(bf16x8& dst, const void* sbase, unsigned voff) {
  asm volatile("global_load_dwordx4 %0, %1, %2" : "=v"(dst) : "v"(voff), "s"(sbase) : "memory");
}

template <bool STATS>
DEV void k3_main(lds_u8* smem, const u16* wp, int ft, int w, int lane, float* stl, f32x16* acc) {
  const int hf = lane >> 5, q4 = (lane & 15) >> 2;
  {
    const int bsl = 2 * ((lane >> 4) & 1) + ((lane & 3) >> 1);
    int boff[4];
#pragma unroll
    for (int i = 0; i < 4; ++i)
      boff[i] = (8 * hf + q4) * 256 + (((4 * ((i + w) & 3) + bsl) ^ (q4 << 2)) * 16) + (lane & 1) * 8;
    const char* wbase = (const char*)(wp + ((size_t)ft * 7 + w) * 32 * 512);
    const unsigned voffW = lane * 16;
    bf16x8 wf[32];
#pragma unroll
    for (int i = 0; i < 12; ++i) gload16(wf[i], wbase + i * 1024, voffW);
#pragma unroll
    for (int j = 0; j < 4; ++j)
#pragma unroll
      for (int i = 0; i < 16; ++i) acc[j][i] = 0.f;
    float sum0 = 0.f, sum1 = 0.f, sq0 = 0.f, sq1 = 0.f;
    bf16x8 b0[4], b1[4];
    auto ld = [&](int kg, bf16x8* bb) {
#pragma unroll
      for (int i = 0; i < 4; ++i) bb[i] = tr_frag(smem + kg * 4096 + boff[i], 1024);
    };
    auto mm = [&](int kg, const bf16x8* bb) {
#pragma unroll
      for (int i = 0; i < 4; ++i) acc[i] = __builtin_amdgcn_mfma_f32_32x32x16_bf16(wf[kg], bb[i], acc[i], 0, 0, 0);
      if (STATS) {
        const u32x4 bu = __builtin_bit_cast(u32x4, bb[0]);
#pragma unroll
        for (int i = 0; i < 4; ++i) {
          const unsigned xi = bu[i];
          const float lo = bflo(xi), hi = bfhi(xi);
          sum0 += lo; sum1 += hi;
          sq0 = fmaf(lo, lo, sq0); sq1 = fmaf(hi, hi, sq1);
        }
        asm volatile("" : "+v"(sum0), "+v"(sum1), "+v"(sq0), "+v"(sq1));
      }
    };
    asm volatile("s_waitcnt vmcnt(8)" : "+v"(wf[0]), "+v"(wf[1]), "+v"(wf[2]), "+v"(wf[3]) :: "memory");
    __builtin_amdgcn_s_barrier();
    ld(0, b0);
#pragma unroll
    for (int c = 0; c < 8; ++c) {
      if (c < 5) {
#pragma unroll
        for (int i = 0; i < 4; ++i) gload16(wf[4 * c + 12 + i], wbase + (4 * c + 12 + i) * 1024, voffW);
      }
      ld(4 * c + 1, b1); mm(4 * c, b0);
      ld(4 * c + 2, b0); mm(4 * c + 1, b1);
      ld(4 * c + 3, b1); mm(4 * c + 2, b0);
      if (c < 7) {
        asm volatile("s_waitcnt vmcnt(%4)" : "+v"(wf[4 * c + 4]), "+v"(wf[4 * c + 5]), "+v"(wf[4 * c + 6]), "+v"(wf[4 * c + 7])
                     : "i"(c < 5 ? 8 : (c == 5 ? 4 : 0)) : "memory");
        __builtin_amdgcn_s_barrier();
        ld(4 * c + 4, b0);
      }
      mm(4 * c + 3, b1);
    }
    if (STATS) {
      float sum = sum0 + sum1, sq = sq0 + sq1;
      sum += __shfl_xor(sum, 32);
      sq += __shfl_xor(sq, 32);
      const float mean = sum * (1.0f / D_);
      const float var = rbf(fmaxf(sq * (1.0f / D_) - mean * mean, 0.f));
      const float inv_ = 1.0f / rbf(sqrtf(rbf(var + 1e-5f)));
      if (lane < 32) *(f32x2*)(stl + (w * 32 + lane) * 2) = (f32x2){inv_, -rbf(mean) * inv_};
    }
  }
}

__global__ __launch_bounds__(512) void k3_gemm1(const u16* __restrict__ hT, const u16* __restrict__ wp,
                                                const float* __restrict__ csp, u16* __restrict__ hh,
                                                const u16* __restrict__ wd) {
  __shared__ __attribute__((aligned(16))) unsigned char smem_[131072 + 2048 + 7168];
  lds_u8* smem = (lds_u8*)smem_;
  const unsigned sbase = (unsigned)(size_t)smem;
  const int t = threadIdx.x, lane = t & 63;
  const int w = __builtin_amdgcn_readfirstlane(t >> 6);
  const int b = blockIdx.x, x = b & 7, idx = b >> 3;
  const int mt = (x & 1) * 8 + (idx & 7), ft = (x >> 1) * 4 + (idx >> 3);
  const int m0 = mt * 128, f0 = ft * 112;
  float* csl = (float*)(smem_ + 131072);
  float* stl = csl + 224;
  const int hf = lane >> 5;
  f32x16 acc[4];
  if (w < 4) {
    k3_main<true>(smem, wp, ft, w, lane, stl, acc);
  } else if (w < 7) {
    k3_main<false>(smem, wp, ft, w, lane, stl, acc);
  } else {
    const unsigned voffA = ((lane >> 4) * S_ + (((lane & 15) ^ ((lane >> 4) << 2)) * 8)) * 2;
    const char* abase = (const char*)(hT + m0);
    auto issue = [&](int c) {
#pragma unroll
      for (int i = 0; i < 16; ++i) glds16(abase + (size_t)(c * 64 + 4 * i) * S_ * 2, voffA, sbase + c * 16384 + i * 1024);
    };
    bf16x8 cpart[8];
    const unsigned voffC = (lane < 56 ? lane : 55) * 16;
    issue(0);
#pragma unroll
    for (int k8 = 0; k8 < 8; ++k8) gload16(cpart[k8], (const char*)(csp + (k8 * 16 + ft) * 224), voffC);
    issue(1);
    const char* pf = (const char*)wd + idx * 57344;
    auto prefetch = [&](int p0, int n) {
      for (int i = 0; i < n; ++i) glds16(pf + (p0 + i) * 1024, lane * 16, sbase + 131072 + 4096);
    };
#pragma unroll 1
    for (int c = 0; c < 8; ++c) {
      if (c == 0) WAITV(16); else if (c < 6) WAITV(32); else if (c == 6) WAITV(30); else WAITV(28);
      __builtin_amdgcn_s_barrier();
      if (c == 0) issue(2);
      if (c < 5) issue(c + 3);
      if (c == 5 || c == 6) prefetch((c - 5) * 14, 14);
    }
    prefetch(28, 28);
    WAITV(0);
    asm volatile("" : "+v"(cpart[0]), "+v"(cpart[1]), "+v"(cpart[2]), "+v"(cpart[3]),
                      "+v"(cpart[4]), "+v"(cpart[5]), "+v"(cpart[6]), "+v"(cpart[7]));
    f32x4 cs4 = __builtin_bit_cast(f32x4, cpart[0]);
#pragma unroll
    for (int k8 = 1; k8 < 8; ++k8) cs4 += __builtin_bit_cast(f32x4, cpart[k8]);
    if (lane < 56) *(f32x4*)(csl + lane * 4) = cs4;
  }
  __syncthreads();
  if (w < 7) {
    f32x4 cg[2], cu[2];
#pragma unroll
    for (int g = 0; g < 2; ++g) {
      cg[g] = *(const f32x4*)(csl + 16 * w + 8 * g + 4 * hf);
      cu[g] = *(const f32x4*)(csl + 112 + 16 * w + 8 * g + 4 * hf);
    }
    u16* orow = hh + (size_t)(m0 + (lane & 31)) * F_ + f0 + 16 * w + 8 * hf;
#pragma unroll
    for (int i = 0; i < 4; ++i) {
      const int mb = (i + w) & 3;
      const f32x2 st2 = *(const f32x2*)(stl + (mb * 32 + (lane & 31)) * 2);
      const float inv = st2[0], mi = st2[1];
      unsigned o[4];
#pragma unroll
      for (int g = 0; g < 2; ++g) {
#pragma unroll
        for (int r = 0; r < 2; ++r) {
          const f32x2 ag = {acc[i][4 * g + 2 * r], acc[i][4 * g + 2 * r + 1]};
          const f32x2 au = {acc[i][8 + 4 * g + 2 * r], acc[i][8 + 4 * g + 2 * r + 1]};
          f32x2 gt = ag * inv + (f32x2){cg[g][2 * r], cg[g][2 * r + 1]} * mi;
          const f32x2 up = au * inv + (f32x2){cu[g][2 * r], cu[g][2 * r + 1]} * mi;
          gt[0] = gt[0] > 0.f ? gt[0] : 0.f;
          gt[1] = gt[1] > 0.f ? gt[1] : 0.f;
          o[2 * g + r] = pk2(gt * up);
        }
      }
      auto s0 = __builtin_amdgcn_permlane32_swap(o[0], o[2], false, false);
      auto s1 = __builtin_amdgcn_permlane32_swap(o[1], o[3], false, false);
      u32x4 ov = {s0[0], s1[0], s0[1], s1[1]};
      *(u32x4*)(orow + (size_t)mb * 32 * F_) = ov;
    }
  }
}

static constexpr int K4_STAGE = 32768;

__global__ __launch_bounds__(384) void k4_gemm2(const u16* __restrict__ hh, const u16* __restrict__ wd,
                                                const u16* __restrict__ hT, u16* __restrict__ out) {
  __shared__ __attribute__((aligned(16))) unsigned char smem_[4 * K4_STAGE];
  lds_u8* smem = (lds_u8*)smem_;
  const int t = threadIdx.x, lane = t & 63;
  const int w = __builtin_amdgcn_readfirstlane(t >> 6);
  const int b = blockIdx.x, x = b & 7, idx = b >> 3;
  const int mt = x * 4 + (idx & 3), nt = idx >> 2;
  const int m0 = mt * 64, n0 = nt * 64;
  const int hf = lane >> 5, r32 = lane & 31;
  if (w >= 4) {
    const unsigned sbase = (unsigned)(size_t)smem + (w - 4) * 16384;
    unsigned voff[4];
    const char* base;
    size_t pstride, sstride;
    if (w == 4) {
      const int ar = lane >> 4, as = lane & 15;
#pragma unroll
      for (int i = 0; i < 4; ++i) voff[i] = ((4 * i + ar) * F_ + (as ^ (4 * i + ar)) * 8) * 2;
      base = (const char*)(hh + (size_t)m0 * F_);
      pstride = (size_t)16 * F_ * 2; sstride = 256;
    } else {
      const int br = lane >> 3, bs = lane & 7;
#pragma unroll
      for (int i = 0; i < 4; ++i) voff[i] = ((8 * i + br) * D_ + (bs ^ (((br >> 1) & 1) << 2)) * 8) * 2;
      base = (const char*)(wd + n0);
      pstride = (size_t)32 * D_ * 2; sstride = (size_t)128 * D_ * 2;
    }
    auto issue = [&](int st) {
      const unsigned sb = sbase + (st & 3) * K4_STAGE;
      const char* pp = base + st * sstride;
#pragma unroll
      for (int i = 0; i < 16; ++i) glds16(pp + (i >> 2) * pstride, voff[i & 3], sb + i * 1024);
    };
    issue(0); issue(1);
#pragma unroll 1
    for (int st = 0; st < 14; ++st) {
      if (st == 0 || st == 12) WAITV(16); else if (st < 12) WAITV(32); else WAITV(0);
      __builtin_amdgcn_s_barrier();
      if (st == 0) issue(2);
      if (st + 3 < 14) issue(st + 3);
    }
  } else {
    const int mb = w >> 1, nb = w & 1;
    u32x2 hr[4];
#pragma unroll
    for (int g = 0; g < 4; ++g) hr[g] = *(const u32x2*)(hT + (n0 + 32 * nb + r32) * S_ + m0 + 32 * mb + 8 * g + 4 * hf);
    f32x16 acc;
#pragma unroll
    for (int e = 0; e < 16; ++e) acc[e] = 0.f;
    const int bq = (lane & 15) >> 2;
    const int bsw = ((bq >> 1) & 1) << 2;
    const int bcol = 32 * nb + 16 * ((lane >> 4) & 1) + 4 * (lane & 3);
    const int boff = 16384 + (8 * hf + bq) * 128 + (((bcol >> 3) ^ bsw) * 16) + (bcol & 7) * 2;
    const int arow = (32 * mb + r32) * 256;
    const int asw = r32 & 15;
#pragma unroll 1
    for (int st = 0; st < 14; ++st) {
      __builtin_amdgcn_s_barrier();
      const lds_u8* sb = smem + (st & 3) * K4_STAGE;
      bf16x8 af[8], bf[8];
#pragma unroll
      for (int ks = 0; ks < 8; ++ks) {
        af[ks] = *(const __attribute__((address_space(3))) bf16x8*)(sb + arow + (((2 * ks + hf) ^ asw) * 16));
        bf[ks] = tr_frag(sb + boff + ks * 16 * 128, 4 * 128);
      }
#pragma unroll
      for (int ks = 0; ks < 8; ++ks) acc = __builtin_amdgcn_mfma_f32_32x32x16_bf16(af[ks], bf[ks], acc, 0, 0, 0);
    }
    u16* ob = (u16*)(smem_ + 2 * K4_STAGE + w * 4096);
#pragma unroll
    for (int g = 0; g < 4; ++g) {
      const unsigned o0 = pk2(unpk(hr[g][0]) + rbf2((f32x2){acc[4 * g], acc[4 * g + 1]}));
      const unsigned o1 = pk2(unpk(hr[g][1]) + rbf2((f32x2){acc[4 * g + 2], acc[4 * g + 3]}));
      u16* op = ob + (8 * g + 4 * hf) * 40 + r32;
      op[0] = (u16)o0; op[40] = (u16)(o0 >> 16); op[80] = (u16)o1; op[120] = (u16)(o1 >> 16);
    }
    __builtin_amdgcn_wave_barrier();
    asm volatile("s_waitcnt lgkmcnt(0)" ::: "memory");
    const int row = lane >> 1, hc = (lane & 1) * 16;
    const u32x4 v0 = *(const u32x4*)(ob + row * 40 + hc);
    const u32x4 v1 = *(const u32x4*)(ob + row * 40 + hc + 8);
    u16* dst = out + (m0 + 32 * mb + row) * D_ + n0 + 32 * nb + hc;
    *(u32x4*)(dst) = v0;
    *(u32x4*)(dst + 8) = v1;
  }
}

extern "C" void kernel_launch(void* const* d_in, const int* in_sizes, int n_in, void* d_out, int out_size,
                              void* d_ws, size_t ws_size, hipStream_t stream) {
  const u16* q = (const u16*)d_in[0];
  const u16* k = (const u16*)d_in[1];
  const u16* v = (const u16*)d_in[2];
  const u16* shk = (const u16*)d_in[3];
  const u16* ssmk = (const u16*)d_in[4];
  const u16* ssmD = (const u16*)d_in[5];
  const u16* wg = (const u16*)d_in[7];
  const u16* wu = (const u16*)d_in[8];
  const u16* wd = (const u16*)d_in[9];
  unsigned char* ws = (unsigned char*)d_ws;
  u16* kvR = (u16*)(ws);
  u16* xT = (u16*)(ws + (2u << 20));
  float* qm = (float*)(ws + (4u << 20));
  float* qinv = qm + 2048;
  float* csp = (float*)(ws + (4u << 20) + 65536);
  u16* h = (u16*)(ws + (5u << 20));
  u16* hh = (u16*)(ws + (8u << 20));
  u16* wp = (u16*)(ws + (16u << 20));
  k1_prep<<<384, 512, 0, stream>>>(q, k, v, shk, kvR, xT, qm, qinv);
  k2_conv<<<412, 512, 0, stream>>>(ssmk, ssmD, kvR, xT, qm, qinv, h, wg, wu, wp, csp, wd, (unsigned*)(ws + (24u << 20)));
  k3_gemm1<<<256, 512, 0, stream>>>(h, wp, csp, hh, wd);
  k4_gemm2<<<256, 384, 0, stream>>>(hh, wd, h, (u16*)d_out);
}
```
